```python
import torch

def _softmax(x, axis=-1):
    return torch.softmax(x, dim=axis)
def _gelu(x):
    return torch.nn.functional.gelu(x, approximate="tanh")
def _silu(x):
    return torch.nn.functional.silu(x)
def _sigmoid(x):
    return torch.sigmoid(x)
def _layernorm(x, axis=-1, eps=1e-5):
    m = x.mean(dim=axis, keepdim=True)
    v = x.var(dim=axis, keepdim=True, unbiased=False)
    return (x - m) / torch.sqrt(v + eps)
def _scatter_add(tgt, idx, src, axis=0):
    return tgt.clone().index_add_(axis, idx, src)
def _segment_sum(src, seg, n):
    out = torch.zeros((n,) + src.shape[1:], dtype=src.dtype)
    return out.index_add_(0, seg, src)
def _pad(x, pw):
    flat = tuple(v for pair in reversed(pw) for v in pair)
    return torch.nn.functional.pad(x, flat)

kernel_name = 'sequential_preLN__ssm_conv_s4d__glu_gated_swiglu__prompt'

def setup_inputs():

    _w = (lambda a: (lambda t: t.to(torch.bfloat16) if t.is_floating_point() else t)(torch.as_tensor(a)))
    def _A_setup():
        Bsz, L, D, N = 1, 2048, 640, 192
        return {
            "u": _w((lambda *s: torch.randn(*s, dtype=torch.bfloat16) * 0.05)(Bsz, L, D)),
            "log_A_real": _w((lambda *s: torch.randn(*s, dtype=torch.bfloat16) * 6.4)(D, N) * 0.5),
            "A_imag": _w((lambda *s: torch.randn(*s, dtype=torch.bfloat16) * 0.05)(D, N) * 3.14),
            "C_real": _w((lambda *s: torch.randn(*s, dtype=torch.bfloat16) * 0.05)(D, N) / N ** 0.5),
            "C_imag": _w((lambda *s: torch.randn(*s, dtype=torch.bfloat16) * 0.05)(D, N) / N ** 0.5),
            "log_dt": _w((lambda *s: torch.randn(*s, dtype=torch.bfloat16) * 0.05)(D) * 0.5 - 4.0),
            "D_skip": _w((lambda *s: torch.randn(*s, dtype=torch.bfloat16) * 0.05)(D)),
        }
    _A_in = _A_setup()

    def _B_setup():
        B, S, D, F = 1, 2048, 640, 1536
        return {
            "x": _w((lambda *s: torch.randn(*s, dtype=torch.bfloat16) * 0.05)(B, S, D)),
            "w1": _w((lambda *s: torch.randn(*s, dtype=torch.bfloat16) * 0.4)(D, F) * (2.0 / D) ** 0.5),
            "w3": _w((lambda *s: torch.randn(*s, dtype=torch.bfloat16) * 0.4)(D, F) * (2.0 / D) ** 0.5),
            "w2": _w((lambda *s: torch.randn(*s, dtype=torch.bfloat16) * 0.4)(F, D) * (2.0 / F) ** 0.5),
        }
    _B_in = _B_setup()

    return {
        "A_u": _A_in["u"],
        "A_log_A_real": _A_in["log_A_real"],
        "A_A_imag": _A_in["A_imag"],
        "A_C_real": _A_in["C_real"],
        "A_C_imag": _A_in["C_imag"],
        "A_log_dt": _A_in["log_dt"],
        "A_D_skip": _A_in["D_skip"],
        "B_x": _B_in["x"],
        "B_w1": _B_in["w1"],
        "B_w3": _B_in["w3"],
        "B_w2": _B_in["w2"],
    }

def reference(A_u, A_log_A_real, A_A_imag, A_C_real, A_C_imag, A_log_dt, A_D_skip, B_x, B_w1, B_w3, B_w2):
    def _A_ref(u, log_A_real, A_imag, C_real, C_imag, log_dt, D_skip):
        L = u.shape[1]
        dt = torch.exp(log_dt)
        A = -torch.exp(log_A_real) + 1j * A_imag
        C = (C_real + 1j * C_imag) * dt[:, None]
        pos = (lambda x, dt: x.to(dt))(torch.arange(L), log_dt.dtype)
        P = torch.exp((A * dt[:, None])[:, :, None] * pos[None, None, :])
        K = 2.0 * torch.einsum("dn,dnl->dl", C, P).real
        L2 = 2 * L
        u_t = (lambda x, *d: x.permute(*d))(u, 0, 2, 1)
        Kf = (lambda x, *a, **k: torch.fft.rfft(x.float(), *a, **k))(K, L2)
        Uf = (lambda x, *a, **k: torch.fft.rfft(x.float(), *a, **k))(u_t, L2)
        y = (lambda x, *a, **k: torch.fft.irfft(x, *a, **k).to(torch.bfloat16))(Uf * Kf[None, :, :], L2)[:, :, :L]
        y = (lambda x, *d: x.permute(*d))(y, 0, 2, 1)
        return y + u * D_skip[None, None, :]

    def _B_ref(x, w1, w3, w2):
        gate = torch.einsum("bsd,df->bsf", x, w1)
        up = torch.einsum("bsd,df->bsf", x, w3)
        h = _silu(gate) * up
        out = torch.einsum("bsf,fd->bsd", h, w2)
        return out

    x = A_u
    h = x + _A_ref(_layernorm(x, axis=-1), A_log_A_real, A_A_imag, A_C_real, A_C_imag, A_log_dt, A_D_skip)
    h2 = h + _B_ref(_layernorm(h, axis=-1), B_w1, B_w3, B_w2)
    return h2
```

```cpp
#include <hip/hip_runtime.h>
#include <hip/hip_bf16.h>
#include <stdint.h>

constexpr int L = 2048, D = 640, N = 192, F = 1536;
typedef unsigned short u16;
__device__ __forceinline__ float bf2f(u16 b) { return __builtin_bit_cast(float, (unsigned)b << 16); }
__device__ __forceinline__ u16 f2bf(float f) { __hip_bfloat16 h = __float2bfloat16(f); return __builtin_bit_cast(u16, h); }
__device__ __forceinline__ float rbf(float f) { return bf2f(f2bf(f)); }

__global__ void ln_kernel(const u16* x, u16* u) {
  int row = blockIdx.x; int t = threadIdx.x;
  float v[10]; float s = 0;
  for (int i = 0; i < 10; i++) { v[i] = bf2f(x[row * D + t + 64 * i]); s += v[i]; }
  for (int o = 32; o; o >>= 1) s += __shfl_xor(s, o);
  float m = s / D; float q = 0;
  for (int i = 0; i < 10; i++) { float d = v[i] - m; q += d * d; }
  for (int o = 32; o; o >>= 1) q += __shfl_xor(q, o);
  float var = rbf(q / D); float mb = rbf(m);
  float sd = rbf(sqrtf(rbf(var + 1e-5f)));
  for (int i = 0; i < 10; i++) u[row * D + t + 64 * i] = f2bf(rbf(v[i] - mb) / sd);
}

__global__ void kgen_kernel(const u16* lar, const u16* aim, const u16* cr, const u16* ci, const u16* ldt, float* K) {
  int d = blockIdx.x;
  float dt = rbf(expf(bf2f(ldt[d])));
  for (int l = threadIdx.x; l < L; l += blockDim.x) {
    float p = rbf((float)l);
    float acc = 0;
    for (int n = 0; n < N; n++) {
      float a = -rbf(expf(bf2f(lar[d * N + n]))) * dt;
      float b = bf2f(aim[d * N + n]) * dt;
      float cre = bf2f(cr[d * N + n]) * dt, cim = bf2f(ci[d * N + n]) * dt;
      float e = expf(a * p); float sn, cs; sincosf(b * p, &sn, &cs);
      acc += e * (cre * cs - cim * sn);
    }
    K[d * L + l] = 2.f * acc;
  }
}

__global__ void conv_kernel(const u16* x, const u16* u, const float* K, const u16* dskip, u16* h) {
  int d = blockIdx.x;
  __shared__ float su[L], sk[L];
  for (int l = threadIdx.x; l < L; l += blockDim.x) { su[l] = bf2f(u[l * D + d]); sk[l] = K[d * L + l]; }
  __syncthreads();
  for (int l = threadIdx.x; l < L; l += blockDim.x) {
    float acc = 0;
    for (int s = 0; s <= l; s++) acc += sk[s] * su[l - s];
    float y = rbf(acc);
    float t = rbf(y + rbf(su[l] * bf2f(dskip[d])));
    h[l * D + d] = f2bf(bf2f(x[l * D + d]) + t);
  }
}

__global__ void gemm1_kernel(const u16* hn, const u16* w1, const u16* w3, u16* hid) {
  int row = blockIdx.x;
  __shared__ float sx[D];
  for (int i = threadIdx.x; i < D; i += blockDim.x) sx[i] = bf2f(hn[row * D + i]);
  __syncthreads();
  for (int f = threadIdx.x; f < F; f += blockDim.x) {
    float g = 0, up = 0;
    for (int k = 0; k < D; k++) { g += sx[k] * bf2f(w1[k * F + f]); up += sx[k] * bf2f(w3[k * F + f]); }
    g = rbf(g); up = rbf(up);
    float s = rbf(g / (1.f + expf(-g)));
    hid[row * F + f] = f2bf(s * up);
  }
}

__global__ void gemm2_kernel(const u16* hid, const u16* w2, const u16* h, u16* out) {
  int row = blockIdx.x;
  __shared__ float sx[F];
  for (int i = threadIdx.x; i < F; i += blockDim.x) sx[i] = bf2f(hid[row * F + i]);
  __syncthreads();
  for (int c = threadIdx.x; c < D; c += blockDim.x) {
    float a = 0;
    for (int k = 0; k < F; k++) a += sx[k] * bf2f(w2[k * D + c]);
    out[row * D + c] = f2bf(bf2f(h[row * D + c]) + rbf(a));
  }
}

extern "C" void kernel_launch(void* const* d_in, const int* in_sizes, int n_in,
                              void* d_out, int out_size, void* d_ws, size_t ws_size,
                              hipStream_t stream) {
  const u16* x = (const u16*)d_in[0];
  char* ws = (char*)d_ws;
  u16* u = (u16*)ws; u16* h = (u16*)(ws + (4 << 20)); u16* hn = (u16*)(ws + (8 << 20));
  u16* hid = (u16*)(ws + (12 << 20)); float* K = (float*)(ws + (24 << 20));
  ln_kernel<<<L, 64, 0, stream>>>(x, u);
  kgen_kernel<<<D, 256, 0, stream>>>((const u16*)d_in[1], (const u16*)d_in[2], (const u16*)d_in[3], (const u16*)d_in[4], (const u16*)d_in[5], K);
  conv_kernel<<<D, 256, 0, stream>>>(x, u, K, (const u16*)d_in[6], h);
  ln_kernel<<<L, 64, 0, stream>>>(h, hn);
  gemm1_kernel<<<L, 256, 0, stream>>>(hn, (const u16*)d_in[8], (const u16*)d_in[9], hid);
  gemm2_kernel<<<L, 256, 0, stream>>>(hid, (const u16*)d_in[10], h, (u16*)d_out);
}
```

```cpp
#include <hip/hip_runtime.h>
#include <stdint.h>

typedef unsigned short u16;
typedef unsigned u32;
typedef unsigned long long u64;
typedef u32 u32x2 __attribute__((ext_vector_type(2)));
typedef u32 u32x4 __attribute__((ext_vector_type(4)));
typedef float f32x2 __attribute__((ext_vector_type(2)));
typedef float f32x4 __attribute__((ext_vector_type(4)));
typedef float f32x16 __attribute__((ext_vector_type(16)));
typedef _Float16 f16x2 __attribute__((ext_vector_type(2)));
typedef __bf16 bf16x2 __attribute__((ext_vector_type(2)));
typedef short s16x4 __attribute__((ext_vector_type(4)));
#define LAS __attribute__((address_space(3)))
#define GAS __attribute__((address_space(1)))
#define DI __device__ __forceinline__
#define RLX_AGENT __ATOMIC_RELAXED, __HIP_MEMORY_SCOPE_AGENT
typedef __amdgpu_buffer_rsrc_t rsrc_t;

constexpr int L_ = 2048, D_ = 640, N_ = 192, F_ = 1536;
constexpr int SC1 = 16;
constexpr int CODE_PREFETCH_BYTES = 36864;
constexpr u32 SPIN_CAP = 1u << 18;

constexpr u32 WS_CTRL = 0;
constexpr u32 WS_EPOCH = 0x10000;
constexpr u32 WS_TFLAG = 0x11000;
constexpr u32 WS_XFLAG = 0x18000;
constexpr u32 WS_ST1 = 0x20000;
constexpr u32 WS_W13 = 0x100000;
constexpr u32 WS_W2 = 0x500000;
constexpr u32 WS_HT = 0x700000;
constexpr u32 WS_HID = 0xA00000;
constexpr u32 WS_DBG = 0x1000000;
constexpr u32 WS_XT = 0x1100000;
constexpr u32 WS_BYTES = 0x1400000;

constexpr int TROW = 2352;
constexpr int IMG_CP = 4160, IMG_SZ = 2 * IMG_CP;
constexpr int LD_PX = 3 * IMG_SZ;
constexpr int LD_U = 124800;
constexpr int U_SZ = 2368;
constexpr int LD_XT = LD_U + 6 * U_SZ;
constexpr int LD_KC = LD_XT + 3 * 4096;
constexpr int KC_SZ = 32 * 56;
constexpr int LD_MISC = LD_KC + 3 * KC_SZ;
constexpr int LD_TS = LD_U;
constexpr int LDS_BYTES = 163840;
static_assert(53 * TROW <= LD_U && LD_PX + 98304 <= LD_U, "");
static_assert(LD_TS + 10240 + 2 * 4224 + 5120 <= LD_KC, "");

template <int I> struct IC { static constexpr int value = I; constexpr operator int() const { return I; } };
template <int B, int E, class F> DI void sfor(F&& f) { if constexpr (B < E) { f(IC<B>{}); sfor<B + 1, E>(f); } }
template <int I> DI void ldw(u32x4& dst, u32 voff, const char* base) {
  asm volatile("global_load_dwordx4 %0, %1, %2 offset:%3 sc1" : "=a"(dst) : "v"(voff), "s"(base + (I >> 2) * 4096), "i"((I & 3) * 1024));
}
#define LDW(dst, voff, base, i) ldw<i>(dst, voff, base)
DI float asf(u32 u) { return __builtin_bit_cast(float, u); }
DI u32 asu(float f) { return __builtin_bit_cast(u32, f); }
DI float bflo(u32 u) { return asf(u << 16); }
DI float bfhi(u32 u) { return asf(u & 0xffff0000u); }
DI u32 pkbf(float a, float b) { f32x2 v = {a, b}; bf16x2 h = __builtin_convertvector(v, bf16x2); return __builtin_bit_cast(u32, h); }
DI u32 pkh(float a, float b) { f32x2 v = {a, b}; f16x2 h = __builtin_convertvector(v, f16x2); return __builtin_bit_cast(u32, h); }
DI u32 pkh2(f32x2 v) { f16x2 h = __builtin_convertvector(v, f16x2); return __builtin_bit_cast(u32, h); }
DI float rbf(float a) { return bflo(pkbf(a, a)); }
template <int CTRL> DI float dpp_add(float v) { return v + asf(__builtin_amdgcn_update_dpp(0, asu(v), CTRL, 0xf, 0xf, true)); }
template <int CTRL> DI u32 dpp_max(u32 v) { u32 o = __builtin_amdgcn_update_dpp(0, v, CTRL, 0xf, 0xf, true); return v > o ? v : o; }
DI f32x2 cmul(f32x2 a, f32x2 b, f32x2 bs) { f32x2 t = a.xx * b; return __builtin_elementwise_fma(a.yy, bs, t); }
DI u32x4 trfrag(LAS char* p0, LAS char* p1) {
  u32x2 lo = __builtin_bit_cast(u32x2, __builtin_amdgcn_ds_read_tr16_b64_v4i16((LAS s16x4*)p0));
  u32x2 hi = __builtin_bit_cast(u32x2, __builtin_amdgcn_ds_read_tr16_b64_v4i16((LAS s16x4*)p1));
  return u32x4{lo.x, lo.y, hi.x, hi.y};
}
DI u32x2 tr64(LAS char* p) { return __builtin_bit_cast(u32x2, __builtin_amdgcn_ds_read_tr16_b64_v4i16((LAS s16x4*)p)); }
DI void swap16(u32& a, u32& b) { auto r = __builtin_amdgcn_permlane16_swap(a, b, false, false); a = r[0]; b = r[1]; }
DI void swap32(u32& a, u32& b) { auto r = __builtin_amdgcn_permlane32_swap(a, b, false, false); a = r[0]; b = r[1]; }
#define VMWAIT(n) asm volatile("s_waitcnt vmcnt(%0)" ::"i"(n) : "memory")
#define BARRIER() do { asm volatile("s_waitcnt lgkmcnt(0)" ::: "memory"); __builtin_amdgcn_s_barrier(); asm volatile("" ::: "memory"); } while (0)
#define SCHED() __builtin_amdgcn_sched_barrier(0)

#define DEF_MFMA(name, op, T, CA) \
  template <bool Z> DI void name(T& acc, const u32x4& a, const u32x4& b) { \
    if constexpr (Z) asm volatile(op " %0, %1, %2, 0" : "=&v"(acc) : CA(a), "v"(b)); \
    else asm volatile(op " %0, %1, %2, %0" : "+v"(acc) : CA(a), "v"(b)); }
DEF_MFMA(mf_bfa, "v_mfma_f32_16x16x32_bf16", f32x4, "a")
DEF_MFMA(mf_bfv, "v_mfma_f32_16x16x32_bf16", f32x4, "v")
DEF_MFMA(mf_hv, "v_mfma_f32_16x16x32_f16", f32x4, "v")
DEF_MFMA(mf_h32, "v_mfma_f32_32x32x16_f16", f32x16, "v")
DI u32 poll32(const char* p) { return __hip_atomic_load((GAS u32*)p, RLX_AGENT); }
template <int N> DI void vmwait() { asm volatile("s_waitcnt vmcnt(%0)" ::"i"(N) : "memory"); }
template <int IMM> DI void set_m0(u32 base) { asm volatile("s_add_u32 m0, %0, %1" ::"s"(base), "i"(IMM) : "scc"); }
template <int IMM> DI void set_m0n(u32 base) { asm volatile("s_add_u32 m0, %0, %1\n\ts_nop 0" ::"s"(base), "i"(IMM) : "scc"); }
DI void dma_m0(u32 voff, rsrc_t r) { asm volatile("buffer_load_dwordx4 %0, %1, m0 offen sc1 lds" ::"v"(voff), "s"(r) : "memory"); }
template <int N> DI void lgkwait() { __builtin_amdgcn_s_waitcnt(0xC07F | (N << 8)); }
template <int O0, int O1, int O2, int O3> DI void put12(const u32 (&v)[4][3]) {
  asm volatile("ds_write_addtid_b32 %0 offset:%12\n\tds_write_addtid_b32 %1 offset:%12+256\n\tds_write_addtid_b32 %2 offset:%12+512\n\t"
               "ds_write_addtid_b32 %3 offset:%13\n\tds_write_addtid_b32 %4 offset:%13+256\n\tds_write_addtid_b32 %5 offset:%13+512\n\t"
               "ds_write_addtid_b32 %6 offset:%14\n\tds_write_addtid_b32 %7 offset:%14+256\n\tds_write_addtid_b32 %8 offset:%14+512\n\t"
               "ds_write_addtid_b32 %9 offset:%15\n\tds_write_addtid_b32 %10 offset:%15+256\n\tds_write_addtid_b32 %11 offset:%15+512"
               ::"v"(v[0][0]), "v"(v[0][1]), "v"(v[0][2]), "v"(v[1][0]), "v"(v[1][1]), "v"(v[1][2]), "v"(v[2][0]), "v"(v[2][1]), "v"(v[2][2]),
                 "v"(v[3][0]), "v"(v[3][1]), "v"(v[3][2]), "i"(O0), "i"(O1), "i"(O2), "i"(O3) : "memory");
}
template <int O0> DI void put3(const u32 (&v)[3]) {
  asm volatile("ds_write_addtid_b32 %0 offset:%3\n\tds_write_addtid_b32 %1 offset:%3+256\n\tds_write_addtid_b32 %2 offset:%3+512"
               ::"v"(v[0]), "v"(v[1]), "v"(v[2]), "i"(O0) : "memory");
}
template <class T> DI T opaque(T v) { asm volatile("" : "+v"(v)); return v; }

#define STAMP_AT(i) do {} while (0)
#define STAMP_W(i, t) do {} while (0)
#define PIN(v) do {} while (0)

DI f32x2 cexp(float ap, float bp) {
  const float e = __builtin_amdgcn_exp2f(ap * 1.44269504088896341f);
  const float C_HI = 0.15915494309189535f, C_LO = 6.4206382e-9f;
  const float x = bp * C_HI;
  const float n = __builtin_rintf(x);
  const float f = __builtin_fmaf(bp, C_LO, __builtin_fmaf(bp, C_HI, -n));
  return f32x2{e * __builtin_amdgcn_cosf(f), e * __builtin_amdgcn_sinf(f)};
}

DI u32 kc_index(int s) {
  const int p = (int)rbf((float)s);
  const u32 q = (u32)(p - 1);
  const u32 j = (q >> 3) & 31, sg = (q >> 8) & 7, c = q & 7;
  const u32 base = __builtin_amdgcn_perm(0x13121110u, 0x0E0C0800u, sg | 0x0c0c0c00u);
  const u32 sh = (0x33332210u >> (4 * sg)) & 3;
  u32 off = j * 56 + (base + (c >> sh)) * 2;
  off = s == 0 ? 40u : off;
  off = s < 0 ? 42u : off;
  return off;
}
DI float fexp(float x) { return __builtin_amdgcn_exp2f(x * 1.44269504088896341f); }

#define MAPL 1
constexpr int NW_CONV = 36;

__global__ __launch_bounds__(256, 1) void fused_kernel(const u16* __restrict__ x, const u16* __restrict__ lar, const u16* __restrict__ aim,
    const u16* __restrict__ cre, const u16* __restrict__ cim, const u16* __restrict__ ldt, const u16* __restrict__ dsk,
    const u16* __restrict__ w1, const u16* __restrict__ w3, const u16* __restrict__ w2, u16* __restrict__ out, char* __restrict__ ws) {
  extern __shared__ __attribute__((aligned(16))) char smem[];
  LAS char* const S = (LAS char*)smem;
  u64 pc;
  asm volatile("s_getpc_b64 %0" : "=s"(pc));
  asm volatile("" ::"s"(x), "s"(lar), "s"(aim), "s"(cre), "s"(cim), "s"(ldt), "s"(dsk), "s"(w1), "s"(w3), "s"(w2), "s"(out), "s"(ws));
  const int tid = threadIdx.x, lane = tid & 63, w = __builtin_amdgcn_readfirstlane(tid >> 6);
  const int b = blockIdx.x, xcd = b & 7, slot = b >> 3;
  const int li = lane & 15, lg = lane >> 4;
  const rsrc_t rws = __builtin_amdgcn_make_buffer_rsrc(ws, 0, WS_BYTES, 0x00020000);
  const rsrc_t rx = __builtin_amdgcn_make_buffer_rsrc((void*)x, 0, L_ * D_ * 2, 0x00020000);
  STAMP_AT(0);

  const int pr = xcd * 16 + (slot >> 1), odd = slot & 1;
  const int ch0 = 5 * pr + (odd ? 3 : 0), nch = odd ? 2 : 3;
  const int gr = (xcd >> MAPL) * (2 << MAPL) + (slot >> (4 - MAPL)), gj = (xcd & ((1 << MAPL) - 1)) * (16 >> MAPL) + (slot & ((16 >> MAPL) - 1));
  const int j2 = gj >> 1, sub = gj & 1;

  u32 tag, gidx[9];
  if (w < 3) {
    const int ch = ch0 + (w < nch ? w : 0);
    const rsrc_t r0 = __builtin_amdgcn_make_buffer_rsrc((void*)(lar + ch * N_), 0, N_ * 2, 0x00020000);
    const rsrc_t r1 = __builtin_amdgcn_make_buffer_rsrc((void*)(aim + ch * N_), 0, N_ * 2, 0x00020000);
    const rsrc_t r2 = __builtin_amdgcn_make_buffer_rsrc((void*)(cre + ch * N_), 0, N_ * 2, 0x00020000);
    const rsrc_t r3 = __builtin_amdgcn_make_buffer_rsrc((void*)(cim + ch * N_), 0, N_ * 2, 0x00020000);
    const rsrc_t r4 = __builtin_amdgcn_make_buffer_rsrc((void*)ldt, 0, D_ * 2, 0x00020000);
    const rsrc_t r5 = __builtin_amdgcn_make_buffer_rsrc((void*)dsk, 0, D_ * 2, 0x00020000);
    const u32 ldtv = __builtin_amdgcn_raw_buffer_load_b16(r4, ch * 2, 0, 0);
    const u32 la2 = __builtin_amdgcn_raw_buffer_load_b32(r0, lane * 4, 0, 0), la1 = __builtin_amdgcn_raw_buffer_load_b16(r0, 256 + lane * 2, 0, 0);
    const u32 ai2 = __builtin_amdgcn_raw_buffer_load_b32(r1, lane * 4, 0, 0), ai1 = __builtin_amdgcn_raw_buffer_load_b16(r1, 256 + lane * 2, 0, 0);
    const u32 cr2 = __builtin_amdgcn_raw_buffer_load_b32(r2, lane * 4, 0, 0), cr1 = __builtin_amdgcn_raw_buffer_load_b16(r2, 256 + lane * 2, 0, 0);
    const u32 ci2 = __builtin_amdgcn_raw_buffer_load_b32(r3, lane * 4, 0, 0), ci1 = __builtin_amdgcn_raw_buffer_load_b16(r3, 256 + lane * 2, 0, 0);
    const u32 dsv = __builtin_amdgcn_raw_buffer_load_b16(r5, ch * 2, 0, 0);
    tag = __builtin_amdgcn_raw_buffer_load_b32(rws, WS_EPOCH, 0, SC1);
    const rsrc_t rc = __builtin_amdgcn_make_buffer_rsrc((void*)(pc & ~255ull), 0, CODE_PREFETCH_BYTES, 0x00020000);
    const u32 cpf = __builtin_amdgcn_raw_buffer_load_b32(rc, (((slot & 1) * 3 + w) * 64 + lane) * 128, 0, 0);
    asm volatile("" ::: "memory");
    SCHED();
#pragma unroll
    for (int k = 0; k < 9; k++) gidx[k] = opaque(kc_index(2047 - 8 * tid - k));
    SCHED();
    STAMP_AT(26);
    PIN(ldtv); PIN(la2); PIN(la1); PIN(ai2); PIN(ai1);
    STAMP_AT(27);
    const float dt = rbf(fexp(bflo(ldtv)));
    f32x2 w1v[3], w8v[3], w256v[3], cc[3];
#pragma unroll
    for (int m = 0; m < 3; m++) {
      const float la = m == 0 ? bflo(la2) : (m == 1 ? bfhi(la2) : bflo(la1));
      const float ai = m == 0 ? bflo(ai2) : (m == 1 ? bfhi(ai2) : bflo(ai1));
      const float a = -rbf(fexp(la)) * dt, bb = ai * dt;
      w8v[m] = cexp(8.f * a, 8.f * bb);
      w1v[m] = cexp(a, -bb);
      w256v[m] = cexp(256.f * a, -256.f * bb);
    }
    PIN(w8v[0]); PIN(w8v[1]); PIN(w8v[2]); PIN(w1v[0]); PIN(w1v[1]); PIN(w1v[2]); PIN(w256v[0]); PIN(w256v[1]); PIN(w256v[2]);
    STAMP_AT(14);
    const u32 m0lo = w * 784;
    set_m0n<0>(m0lo);
    {
      f32x2 ws8[3], v[3];
      u32 pk[4][3];
#pragma unroll
      for (int m = 0; m < 3; m++) { ws8[m] = f32x2{-w8v[m].y, w8v[m].x}; v[m] = w8v[m]; pk[0][m] = 0x00003c00u; pk[1][m] = pkh2(v[m]); }
      sfor<0, 8>([&](auto G_) {
        constexpr int g = G_;
#pragma unroll
        for (int i = (g == 0 ? 2 : 0); i < 4; i++)
#pragma unroll
          for (int m = 0; m < 3; m++) { v[m] = cmul(v[m], w8v[m], ws8[m]); pk[i][m] = pkh2(v[m]); }
        if constexpr (g == 6) set_m0n<24 * TROW>(m0lo);
        constexpr int r0_ = g < 6 ? 4 * g : 4 * g - 24;
        put12<r0_ * TROW, (r0_ + 1) * TROW, (r0_ + 2) * TROW, (r0_ + 3) * TROW>(pk);
      });
    }
    STAMP_AT(28);
    u32 cm = cr2 & 0x7fffu;
    { u32 t;
      t = (cr2 >> 16) & 0x7fffu; cm = cm > t ? cm : t;
      t = ci2 & 0x7fffu; cm = cm > t ? cm : t;
      t = (ci2 >> 16) & 0x7fffu; cm = cm > t ? cm : t;
      t = cr1 & 0x7fffu; cm = cm > t ? cm : t;
      t = ci1 & 0x7fffu; cm = cm > t ? cm : t; }
    cm = dpp_max<0xB1>(cm); cm = dpp_max<0x4E>(cm); cm = dpp_max<0x141>(cm); cm = dpp_max<0x140>(cm);
    { u32 a0 = __builtin_amdgcn_readlane(cm, 0), a1 = __builtin_amdgcn_readlane(cm, 16), a2 = __builtin_amdgcn_readlane(cm, 32), a3 = __builtin_amdgcn_readlane(cm, 48);
      a0 = a0 > a1 ? a0 : a1; a2 = a2 > a3 ? a2 : a3; cm = a0 > a2 ? a0 : a2; }
    int ec = (int)(cm >> 7);
    ec = ec < 16 ? 16 : (ec > 254 ? 254 : ec);
    const float s1 = asf((u32)(127 + 13 + 127 - ec) << 23);
    const float s1i = asf((u32)(ec - 13) << 23);
    const float bound = 544.f * dt * bflo(cm) + fabsf(bflo(dsv));
    int eb = (int)(asu(bound) >> 23);
    eb = eb < 16 ? 16 : (eb > 254 ? 254 : eb);
    const float s2 = asf((u32)(127 + 14 + 127 - eb) << 23);
    const float s2i = asf((u32)(eb - 14) << 23);
    cc[0] = f32x2{bflo(cr2) * s1, -bflo(ci2) * s1}; cc[1] = f32x2{bfhi(cr2) * s1, -bfhi(ci2) * s1}; cc[2] = f32x2{bflo(cr1) * s1, -bflo(ci1) * s1};
    set_m0n<27 * TROW>(m0lo);
    {
      f32x2 ws1[3], ws256[3], v[3], k2[3], k4[3], k6[3];
      u32 pk[4][3];
#pragma unroll
      for (int m = 0; m < 3; m++) { ws1[m] = f32x2{-w1v[m].y, w1v[m].x}; ws256[m] = f32x2{-w256v[m].y, w256v[m].x}; v[m] = cc[m]; pk[0][m] = pkh2(v[m]); }
      put3<25 * TROW>(pk[0]);
#pragma unroll
      for (int i = 0; i < 4; i++)
#pragma unroll
        for (int m = 0; m < 3; m++) { v[m] = cmul(v[m], w1v[m], ws1[m]); pk[i][m] = pkh2(v[m]); if (i == 1) k2[m] = v[m]; if (i == 3) k4[m] = v[m]; }
      put12<5 * TROW, 6 * TROW, 7 * TROW, 8 * TROW>(pk);
#pragma unroll
      for (int i = 0; i < 4; i++)
#pragma unroll
        for (int m = 0; m < 3; m++) { v[m] = cmul(v[m], w1v[m], ws1[m]); pk[i][m] = pkh2(v[m]); if (i == 1) k6[m] = v[m]; }
      put12<9 * TROW, 10 * TROW, 11 * TROW, 12 * TROW>(pk);
#pragma unroll
      for (int m = 0; m < 3; m++) {
        k2[m] = cmul(k2[m], w256v[m], ws256[m]); pk[0][m] = pkh2(k2[m]);
        k4[m] = cmul(k4[m], w256v[m], ws256[m]); pk[1][m] = pkh2(k4[m]);
        k6[m] = cmul(k6[m], w256v[m], ws256[m]); pk[2][m] = pkh2(k6[m]);
        v[m] = cmul(v[m], w256v[m], ws256[m]); pk[3][m] = pkh2(v[m]);
      }
      put12<13 * TROW, 14 * TROW, 15 * TROW, 16 * TROW>(pk);
#pragma unroll
      for (int i = 0; i < 2; i++)
#pragma unroll
        for (int m = 0; m < 3; m++) {
          k4[m] = cmul(k4[m], w256v[m], ws256[m]); pk[2 * i][m] = pkh2(k4[m]);
          v[m] = cmul(v[m], w256v[m], ws256[m]); pk[2 * i + 1][m] = pkh2(v[m]);
        }
      put12<17 * TROW, 18 * TROW, 19 * TROW, 20 * TROW>(pk);
#pragma unroll
      for (int i = 0; i < 4; i++)
#pragma unroll
        for (int m = 0; m < 3; m++) { v[m] = cmul(v[m], w256v[m], ws256[m]); pk[i][m] = pkh2(v[m]); }
      put12<21 * TROW, 22 * TROW, 23 * TROW, 24 * TROW>(pk);
    }
    STAMP_AT(15);
    const int rr = lane & 31, hf = lane >> 5;
    LAS char* const pa = S + (32 + (rr > 20 ? 20 : rr)) * TROW + w * 784 + hf * 16;
    LAS char* const pb = S + rr * TROW + w * 784 + hf * 16;
    f32x16 kacc;
    u32x4 fa[24], fb[24];
    sfor<0, 28>([&](auto K_) {
      constexpr int ks = K_;
      if constexpr (ks < 24) { fa[ks] = *(LAS u32x4*)(pa + ks * 32); fb[ks] = *(LAS u32x4*)(pb + ks * 32); }
      if constexpr (ks >= 4) mf_h32<ks == 4>(kacc, fa[ks - 4], fb[ks - 4]);
      SCHED();
    });
    asm volatile("s_nop 7\n\ts_nop 7" : "+v"(kacc));
    const float fac = 2.f * dt * s1i * s2;
    LAS char* const kc = S + LD_KC + w * KC_SZ + rr * 56 + hf * 8;
    const float k8 = kacc[8] * fac + (hf ? bflo(dsv) * s2 : 0.f);
    const u32x2 q0 = {pkh(kacc[0] * fac, kacc[1] * fac), pkh(kacc[2] * fac, kacc[3] * fac)};
    const u32x2 q1 = {pkh(kacc[4] * fac, kacc[5] * fac), pkh(kacc[6] * fac, kacc[7] * fac)};
    const u32x2 q2 = {pkh(k8, hf ? 0.f : kacc[9] * fac), pkh(kacc[10] * fac, kacc[11] * fac)};
    *(LAS u32x2*)(kc) = q0; *(LAS u32x2*)(kc + 16) = q1; *(LAS u32x2*)(kc + 32) = q2;
    *(LAS float*)(S + LD_MISC + w * 4) = s2i;
    asm volatile("" ::"v"(cpf));
  } else {
    __builtin_amdgcn_raw_buffer_store_b32(0u, rws, WS_CTRL + b * 256 + lane * 4, 0, SC1);
    u32x4 xt[10], xr[10];
    int vo10[5];
#pragma unroll
    for (int i = 0; i < 5; i++) { const int id = lane + 64 * i, rho = id / 10, c = id - 10 * rho; vo10[i] = (rho * D_ + 8 * c) * 2; }
#pragma unroll
    for (int i = 0; i < 10; i++) xt[i] = __builtin_amdgcn_raw_buffer_load_b128(rx, vo10[i % 5], ((64 * slot + 32 * (i / 5)) * D_ + 80 * xcd) * 2, 0);
    const int row = 8 * b + (lane >> 3), part = lane & 7;
#pragma unroll
    for (int i = 0; i < 10; i++) xr[i] = __builtin_amdgcn_raw_buffer_load_b128(rx, row * (D_ * 2) + (part + 8 * i) * 16, 0, 0);
    tag = __builtin_amdgcn_raw_buffer_load_b32(rws, WS_EPOCH, 0, SC1);
    asm volatile("" ::: "memory");
    SCHED();
    const rsrc_t rw1 = __builtin_amdgcn_make_buffer_rsrc((void*)w1, 0, D_ * F_ * 2, 0x00020000);
    const rsrc_t rw3 = __builtin_amdgcn_make_buffer_rsrc((void*)w3, 0, D_ * F_ * 2, 0x00020000);
    const rsrc_t rw2 = __builtin_amdgcn_make_buffer_rsrc((void*)w2, 0, D_ * F_ * 2, 0x00020000);
    u32x4 wa[4][4], wb[2][5];
    const bool do13 = gr < 15;
    const int vo13 = ((lane >> 2) * F_ + 8 * (lane & 3)) * 2;
#pragma unroll
    for (int u = 0; u < 4; u++) {
      const int su = do13 ? 4 * gr + u : u, ks = su / 3, t = su - 3 * ks;
      const int so = (32 * ks * F_ + 96 * gj + 32 * t) * 2;
      wa[u][0] = __builtin_amdgcn_raw_buffer_load_b128(rw1, vo13, so, 0); wa[u][1] = __builtin_amdgcn_raw_buffer_load_b128(rw3, vo13, so, 0);
      wa[u][2] = __builtin_amdgcn_raw_buffer_load_b128(rw1, vo13, so + 32 * F_, 0); wa[u][3] = __builtin_amdgcn_raw_buffer_load_b128(rw3, vo13, so + 32 * F_, 0);
    }
#pragma unroll
    for (int i = 0; i < 5; i++) {
#pragma unroll
      for (int u = 0; u < 2; u++) {
        const int un = (u == 1 && b < 128) ? b + 256 : b, uj = un & 7, uk = un >> 3;
        wb[u][i] = __builtin_amdgcn_raw_buffer_load_b128(rw2, vo10[i], (32 * uk * D_ + 80 * uj) * 2, 0);
      }
    }
    asm volatile("" ::: "memory");
    SCHED();
#pragma unroll
    for (int k = 0; k < 9; k++) gidx[k] = opaque(kc_index(2047 - 8 * tid - k));
    SCHED();
    STAMP_W(29, 192);
    PIN(xt[9]);
    STAMP_W(30, 192);
    {
      LAS char* const XS = S + LD_TS;
#pragma unroll
      for (int i = 0; i < 10; i++) *(LAS u32x4*)(XS + (lane + 64 * i) * 16) = xt[i];
      LAS char* const p0 = XS + (8 * (4 * (lg & 1) + (lane & 3)) + ((lane >> 2) & 3)) * 160 + (lg >> 1) * 8;
      u32x4 fr[10];
#pragma unroll
      for (int k = 0; k < 10; k++) fr[k] = trfrag(p0 + k * 16, p0 + 640 + k * 16);
      const int xo = WS_XT + (20 * xcd + (lg >> 1)) * 16384 + slot * 512 + (lane & 31) * 16;
#pragma unroll
      for (int k = 0; k < 10; k++) __builtin_amdgcn_raw_buffer_store_b128(fr[k], rws, xo + k * 32768, 0, SC1);
      asm volatile("" ::: "memory");
      SCHED();
    }
    STAMP_W(24, 192);
    f32x2 xv[40], sm2 = {0.f, 0.f};
#pragma unroll
    for (int i = 0; i < 10; i++)
#pragma unroll
      for (int e = 0; e < 4; e++) { xv[4 * i + e] = f32x2{bflo(xr[i][e]), bfhi(xr[i][e])}; sm2 += xv[4 * i + e]; }
    float sm = sm2.x + sm2.y;
    sm = dpp_add<0xB1>(sm); sm = dpp_add<0x4E>(sm); sm = dpp_add<0x141>(sm);
    const float mean = sm * (1.f / D_);
    f32x2 q2 = {0.f, 0.f};
#pragma unroll
    for (int i = 0; i < 40; i++) { const f32x2 d = xv[i] - mean; q2 = __builtin_elementwise_fma(d, d, q2); }
    float q = q2.x + q2.y;
    q = dpp_add<0xB1>(q); q = dpp_add<0x4E>(q); q = dpp_add<0x141>(q);
    const float sd = __builtin_amdgcn_sqrtf(rbf(rbf(q * (1.f / D_)) + 1e-5f));
    if (part == 0) __builtin_amdgcn_raw_buffer_store_b64(u32x2{pkbf(mean, sd), tag + 1u}, rws, WS_ST1 + row * 8, 0, SC1);
    asm volatile("" ::: "memory");
    STAMP_W(31, 192);
    VMWAIT(1);
    if (lane == 0) __builtin_amdgcn_raw_buffer_store_b32(tag + 1u, rws, WS_XFLAG + (xcd * 32 + slot) * 128, 0, SC1);
    STAMP_W(16, 192);
    LAS char* TS = S + LD_TS + 10240;
    if (do13) {
#pragma unroll
      for (int u = 0; u < 4; u++) {
        const int su = 4 * gr + u, ks = su / 3, t = su - 3 * ks;
        TS = S + LD_TS + 10240 + (u & 1) * 4224;
        LAS char* const pw = TS + (lane & 3) * 1056 + (lane >> 2) * 32;
#pragma unroll
        for (int hh = 0; hh < 2; hh++) {
          const u32x4 g = wa[u][2 * hh], up = wa[u][2 * hh + 1];
          *(LAS u32x4*)(pw + hh * 512) = u32x4{g.x, up.x, g.y, up.y};
          *(LAS u32x4*)(pw + hh * 512 + 16) = u32x4{g.z, up.z, g.w, up.w};
        }
#pragma unroll
        for (int f = 0; f < 4; f++) {
          const u32x4 fr = trfrag(TS + f * 1056 + lane * 8, TS + f * 1056 + 512 + lane * 8);
          __builtin_amdgcn_raw_buffer_store_b128(fr, rws, lane * 16 + WS_W13 + (((gj * 4 + f) * 20 + ks) * 3 + t) * 1024, 0, SC1);
        }
      }
    }
#pragma unroll
    for (int u = 0; u < 2; u++) {
      if (u == 0 || b < 128) {
        const int un = u == 1 ? b + 256 : b, uj = un & 7, uk = un >> 3;
        TS = u == 0 ? S + LD_TS + 10240 + 2 * 4224 : S + LD_TS;
#pragma unroll
        for (int i = 0; i < 5; i++) *(LAS u32x4*)(TS + (lane + 64 * i) * 16) = wb[u][i];
        LAS char* const pr0 = TS + (8 * lg + ((lane >> 2) & 3)) * 160;
#pragma unroll
        for (int t = 0; t < 5; t++) {
          const int col = t < 4 ? 32 * (t >> 1) + 8 * (lane & 3) + 4 * (t & 1) : 64 + 4 * (lane & 3);
          const u32x4 fr = trfrag(pr0 + col * 2, pr0 + 640 + col * 2);
          __builtin_amdgcn_raw_buffer_store_b128(fr, rws, lane * 16 + WS_W2 + ((uj * 48 + uk) * 5 + t) * 1024, 0, SC1);
        }
      }
    }
    STAMP_W(17, 192);
    VMWAIT(0);
    if (lane == 0) __builtin_amdgcn_raw_buffer_store_b32(tag + 1u, rws, WS_TFLAG + gj * 256 + gr * 4, 0, SC1);
    STAMP_W(18, 192);
  }
  tag += 1u;
  STAMP_AT(1);
  u32x4 st[4];
#pragma unroll
  for (int i = 0; i < 4; i++) st[i] = __builtin_amdgcn_raw_buffer_load_b128(rws, WS_ST1 + tid * 64 + i * 16, 0, SC1);
  u32 xf = poll32(ws + WS_XFLAG + (xcd * 32 + w * 8 + (lane & 7)) * 128);
  BARRIER();
  STAMP_AT(2);
  for (u32 spins = 0;; spins++) {
    if (__all(xf == tag) || spins > SPIN_CAP) break;
    xf = poll32(ws + WS_XFLAG + (xcd * 32 + w * 8 + (lane & 7)) * 128);
  }
  asm volatile("" ::: "memory");
  u32x4 xc[3];
#pragma unroll
  for (int c = 0; c < 3; c++) { const int ch = ch0 + (c < nch ? c : 0); xc[c] = __builtin_amdgcn_raw_buffer_load_b128(rws, tid * 64, WS_XT + (ch >> 2) * 16384 + (ch & 3) * 16, SC1); }
  STAMP_AT(25);
  {
    u32 e[3][9];
#pragma unroll
    for (int c = 0; c < 3; c++)
#pragma unroll
      for (int k = 0; k < 9; k++) e[c][k] = *(LAS u16*)(S + LD_KC + c * KC_SZ + gidx[k]);
    SCHED();
    u32x4 p1[3], p0[3];
#pragma unroll
    for (int c = 0; c < 3; c++)
#pragma unroll
      for (int n = 0; n < 4; n++) { p1[c][n] = e[c][2 * n] | (e[c][2 * n + 1] << 16); p0[c][n] = e[c][2 * n + 1] | (e[c][2 * n + 2] << 16); }
    SCHED();
#pragma unroll
    for (int c = 0; c < 3; c++) { *(LAS u32x4*)(S + c * IMG_SZ + tid * 16) = p0[c]; *(LAS u32x4*)(S + c * IMG_SZ + IMG_CP + tid * 16) = p1[c]; }
    if (tid < 12) *(LAS u32x4*)(S + (tid >> 1) * IMG_CP + 4096 + (tid & 1) * 16) = u32x4{0, 0, 0, 0};
    if (tid >= 128 && tid < 224) *(LAS u32x4*)(S + LD_U + ((tid - 128) >> 4) * U_SZ + (tid & 15) * 16) = u32x4{0, 0, 0, 0};
  }
  STAMP_AT(3);
  for (u32 spins = 0;; spins++) {
    bool ok = true;
#pragma unroll
    for (int i = 0; i < 4; i++) ok = ok && st[i].y == tag && st[i].w == tag;
    if (__all(ok) || spins > SPIN_CAP) break;
    asm volatile("" ::: "memory");
#pragma unroll
    for (int i = 0; i < 4; i++) st[i] = __builtin_amdgcn_raw_buffer_load_b128(rws, WS_ST1 + tid * 64 + i * 16, 0, SC1);
  }
  u32 tf = poll32(ws + WS_TFLAG + gj * 256 + li * 4);
  STAMP_AT(4);
  {
    float ra[8], rb[8];
#pragma unroll
    for (int i = 0; i < 8; i++) { const u32 pl = (i & 1) ? st[i >> 1].z : st[i >> 1].x; ra[i] = __builtin_amdgcn_rcpf(bfhi(pl)); rb[i] = -bflo(pl) * ra[i]; }
    u32x4 v[3];
#pragma unroll
    for (int c = 0; c < 3; c++)
#pragma unroll
      for (int i = 0; i < 4; i++) {
        v[c][i] = pkh(fmaf(bflo(xc[c][i]), ra[2 * i], rb[2 * i]), fmaf(bfhi(xc[c][i]), ra[2 * i + 1], rb[2 * i + 1]));
      }
    SCHED();
#pragma unroll
    for (int c = 0; c < 3; c++) *(LAS u32x4*)(S + LD_U + (2 * c + (tid & 1)) * U_SZ + ((tid >> 1) + 16) * 16) = v[c];
#pragma unroll
    for (int c = 0; c < 3; c++) *(LAS u32x4*)(S + LD_XT + c * 4096 + tid * 16) = xc[c];
  }
  LAS char* hb[3][4];
  {
    LAS char* const h0 = S + (li & 1) * IMG_CP + 4092 - 32 * lg - 4 * (li >> 1) - 128 * w - 512;
#pragma unroll
    for (int c = 0; c < 3; c++)
#pragma unroll
      for (int i = 0; i < 4; i++) hb[c][i] = opaque(h0 + c * IMG_SZ - 1024 * i);
  }
  LAS char* const gb = opaque(S + LD_U + (li - lg + 16) * 16 - 64 * w);
  LAS char* const pxw = opaque(S + LD_PX + w * 2048 + lane * 16);
  LAS char* const pxw2 = opaque(pxw + 65536);
  const u32 voff = lane * 16;
  const char* const wp = ws + WS_W13 + (gj * 4 + w) * 61440;
  BARRIER();
  if (b == 0 && tid == 0) __builtin_amdgcn_raw_buffer_store_b32(tag, rws, WS_EPOCH, 0, SC1);
  STAMP_AT(5);
  u32x4 W[60];
  {
    u32x2 Hl[2][8], Hh[2][8];
    u32x4 G[2][8];
    auto rd = [&](auto C_, auto HH_, auto N_) {
      constexpr int c = C_, hh = HH_, n = N_, i = n / 3, k = n % 3;
      if constexpr (k == 0) G[hh][i] = *(LAS u32x4*)(gb + (2 * c + hh) * U_SZ + 256 * i);
      else {
        LAS u32* const p = (LAS u32*)(hb[c][i >> 1] + ((i & 1) ? 0 : 512) + 16 * hh + (k == 2 ? 8 : 0));
        const u32x2 v = {p[0], p[1]};
        if constexpr (k == 1) Hl[hh][i] = v; else Hh[hh][i] = v;
      }
    };
    sfor<0, 24>([&](auto N_) { rd(IC<0>{}, IC<0>{}, N_); });
    for (u32 spins = 0;; spins++) {
      if (__all(tf == tag) || spins > SPIN_CAP) break;
      tf = poll32(ws + WS_TFLAG + gj * 256 + li * 4);
    }
    asm volatile("" ::: "memory");
    STAMP_AT(19);
    f32x4 last7;
    auto channel = [&](auto C_) {
      constexpr int c = C_;
      f32x4 acc[8];
      auto putacc = [&](int cc, int al, const f32x4& v) {
        LAS char* const p = (cc == 2 ? pxw2 : pxw + cc * 32768) + (al >> 1) * 8192 + (al & 1) * 1024;
        *(LAS f32x4*)p = v;
      };
      sfor<0, 2>([&](auto HH_) {
        constexpr int hh = HH_;
        lgkwait<0>();
        SCHED();
        sfor<0, 36>([&](auto N_) {
          constexpr int n = N_;
          constexpr int al = n < 1 ? 0 : n < 3 ? 1 : n < 6 ? 2 : n < 10 ? 3 : n < 15 ? 4 : n < 21 ? 5 : n < 28 ? 6 : 7;
          constexpr int m1 = n - al * (al + 1) / 2;
          mf_hv<hh == 0 && m1 == 0>(acc[al], u32x4{Hl[hh][m1].x, Hl[hh][m1].y, Hh[hh][m1].x, Hh[hh][m1].y}, G[hh][al - m1]);
          if constexpr (n < 24) {
            if constexpr (hh == 0) rd(C_, IC<1>{}, N_);
            else if constexpr (c < 2) rd(IC<c + 1>{}, IC<0>{}, N_);
          }
          if constexpr (c < 2 && (n & 3) == 1) { constexpr int i = (2 * c + hh) * 9 + (n >> 2); LDW(W[i], voff, wp, i); }
          if constexpr (hh == 0 && c > 0 && n == 6) putacc(c - 1, 7, last7);
          if constexpr (hh == 1) {
            if constexpr (n == 10) putacc(c, 0, acc[0]);
            if constexpr (n == 14) putacc(c, 1, acc[1]);
            if constexpr (n == 18) putacc(c, 2, acc[2]);
            if constexpr (n == 22) putacc(c, 3, acc[3]);
            if constexpr (n == 26) putacc(c, 4, acc[4]);
            if constexpr (n == 30) putacc(c, 5, acc[5]);
            if constexpr (n == 35) putacc(c, 6, acc[6]);
          }
          SCHED();
        });
      });
      last7 = acc[7];
    };
    channel(IC<0>{});
    channel(IC<1>{});
    if (nch == 3) {
      channel(IC<2>{});
      asm volatile("s_nop 7" ::"v"(last7) : "memory");
      *(LAS f32x4*)(pxw2 + 3 * 8192 + 1024) = last7;
    } else {
      asm volatile("s_nop 7" ::"v"(last7) : "memory");
      *(LAS f32x4*)(pxw + 32768 + 3 * 8192 + 1024) = last7;
    }
  }
  BARRIER();
  STAMP_AT(6);
  {
    f32x4 pv[3][2][4];
    u32x2 xv[3][2];
    float inv[3];
#pragma unroll
    for (int c = 0; c < 3; c++) {
      inv[c] = *(LAS float*)(S + LD_MISC + c * 4);
#pragma unroll
      for (int t = 0; t < 2; t++) {
        LAS char* const p = S + LD_PX + c * 32768 + w * 8192 + t * 1024 + lane * 16;
#pragma unroll
        for (int v = 0; v < 4; v++) pv[c][t][v] = *(LAS f32x4*)(p + v * 2048);
        xv[c][t] = *(LAS u32x2*)(S + LD_XT + c * 4096 + 512 * (2 * w + t) + 32 * li + 8 * lg);
      }
    }
    SCHED();
    const int a = 16 * (2 * w + (lg & 1)) + li;
#pragma unroll
    for (int c = 0; c < 3; c++) {
      u32 o[2][2];
#pragma unroll
      for (int t = 0; t < 2; t++) {
        const f32x4 y = (pv[c][t][0] + pv[c][t][1]) + (pv[c][t][2] + pv[c][t][3]);
        o[t][0] = pkbf(fmaf(y[0], inv[c], bflo(xv[c][t].x)), fmaf(y[1], inv[c], bfhi(xv[c][t].x)));
        o[t][1] = pkbf(fmaf(y[2], inv[c], bflo(xv[c][t].y)), fmaf(y[3], inv[c], bfhi(xv[c][t].y)));
      }
      swap16(o[0][0], o[1][0]); swap16(o[0][1], o[1][1]);
      const int d = ch0 + c;
      const int off = (a >> 2) * 81920 + (((a & 3) ^ ((d >> 1) & 3)) * 32) + (lg >> 1) * 16;
      if (c < nch) __builtin_amdgcn_raw_buffer_store_b128(u32x4{o[0][0], o[0][1], o[1][0], o[1][1]}, rws, off + WS_HT + d * 128, 0, SC1);
    }
  }
  STAMP_AT(20);
  VMWAIT(0);
  BARRIER();
  if (tid < 8) __hip_atomic_fetch_add((GAS u32*)(ws + WS_CTRL + (tid * 8 + (slot & 7)) * 256), 1u, RLX_AGENT);
  STAMP_AT(7);
  sfor<NW_CONV, 60>([&](auto I) { constexpr int i = I; LDW(W[i], voff, wp, i); });
  f32x4 cs[3];
  const u32x4 on = opaque(u32x4{0x3f803f80u, 0x3f803f80u, 0x3f803f80u, 0x3f803f80u});
  const rsrc_t rht = __builtin_amdgcn_make_buffer_rsrc(ws + WS_HT + gr * 163840, 0, 163840, 0x00020000);
  LAS char* bp[3][4];
#pragma unroll
  for (int m = 0; m < 4; m++) {
    LAS char* const p = S + (lane >> 2) * 128 + (((m ^ w) ^ ((lane >> 3) & 3)) * 32) + (lane & 3) * 8;
#pragma unroll
    for (int k = 0; k < 3; k++) bp[k][m] = opaque(p + k * 65536);
  }
  asm volatile("s_nop 1");
  sfor<0, NW_CONV>([&](auto I) { constexpr int i = I; mf_bfa<(i < 3)>(cs[i % 3], W[i], on); });
  VMWAIT(0);
  sfor<NW_CONV, 60>([&](auto I) { constexpr int i = I; mf_bfa<false>(cs[i % 3], W[i], on); });
  STAMP_AT(21);
  for (u32 spins = 0;; spins++) {
    const u32 f = poll32(ws + WS_CTRL + (xcd * 8 + (lane & 7)) * 256);
    if (__all(f >= 32u) || spins > SPIN_CAP) break;
  }
  asm volatile("" ::: "memory");
  STAMP_AT(8);
  f32x4 acc1[2][4][3], sq[2], su[2];
  u32x4 V[60];
  {
    const u32 m0w = w * 1024;
    const char* const vp = ws + WS_W2 + (j2 * 4 + w) * 61440;
    sfor<0, 10>([&](auto I) { constexpr int s = I; set_m0n<s * 4096>(m0w); dma_m0(voff, rht); });
    u32x2 Bl[8], Bh[8];
    auto rdb = [&](auto F_, auto H_) {
      constexpr int f = F_, h = H_, s = f >> 2, m = f & 3, o = s * 4096 + h * 2048;
      const u32x2 v = tr64(bp[o >> 16][m] + (o & 65535));
      if constexpr (h == 0) Bl[f & 7] = v; else Bh[f & 7] = v;
    };
    vmwait<8>();
    __builtin_amdgcn_s_barrier();
    asm volatile("" ::: "memory");
    STAMP_AT(22);
    sfor<0, 6>([&](auto F_) { rdb(F_, IC<0>{}); rdb(F_, IC<1>{}); });
    sfor<0, 40>([&](auto S_) {
      constexpr int s = S_, hf = s / 20, ks = s % 20;
      lgkwait<(s < 39 ? 4 : 0)>();
      SCHED();
      sfor<0, 4>([&](auto M_) {
        constexpr int m = M_, f = 4 * s + m;
        if constexpr (m == 2) {
          if constexpr (s == 0) { vmwait<11 - 6>(); __builtin_amdgcn_s_barrier(); }
          if constexpr (s == 4) { vmwait<19 - 12>(); __builtin_amdgcn_s_barrier(); }
          if constexpr (s == 10) { vmwait<31 - 20>(); __builtin_amdgcn_s_barrier(); }
          if constexpr (s == 18) { vmwait<0>(); __builtin_amdgcn_s_barrier(); }
          asm volatile("" ::: "memory");
        }
        const u32x4 B = {Bl[f & 7].x, Bl[f & 7].y, Bh[f & 7].x, Bh[f & 7].y};
        mf_bfa<ks == 0>(acc1[hf][m][0], W[ks * 3], B);
        if constexpr (f + 6 < 160) rdb(IC<f + 6>{}, IC<0>{});
        if constexpr (s < 15 && (m & 1)) set_m0<(10 + 2 * s + (m >> 1)) * 4096>(m0w);
        SCHED();
        mf_bfa<ks == 0>(acc1[hf][m][1], W[ks * 3 + 1], B);
        if constexpr (f + 6 < 160) rdb(IC<f + 6>{}, IC<1>{});
        SCHED();
        mf_bfa<ks == 0>(acc1[hf][m][2], W[ks * 3 + 2], B);
        if constexpr (s < 15 && (m & 1)) dma_m0(voff, rht);
        if constexpr (s > 20 && m < 3) { constexpr int i = (s - 21) * 3 + m; LDW(V[i], voff, vp, i); }
        if constexpr (m == 0) { mf_bfv<ks == 0>(sq[hf], B, B); mf_bfv<ks == 0>(su[hf], on, B); }
        SCHED();
      });
    });
    sfor<57, 60>([&](auto I) { constexpr int i = I; LDW(V[i], voff, vp, i); });
  }
  STAMP_AT(9);
  u32x2 res[5];
  {
    const int hfw = w >> 1, mfw = 2 * sub + (w & 1);
#pragma unroll
    for (int t = 0; t < 5; t++) {
      const int row = 80 * j2 + (t < 4 ? 32 * (t >> 1) + 8 * lg + 4 * (t & 1) : 64 + 4 * lg) + ((lane >> 2) & 3);
      res[t] = tr64(S + hfw * 81920 + row * 128 + ((mfw ^ ((row >> 1) & 3)) * 32) + (lane & 3) * 8);
    }
  }
  LAS char* const stp = S + (j2 == 0 ? 40960 : 0);
  asm volatile("s_nop 7" : "+v"(sq[0]), "+v"(sq[1]), "+v"(su[0]), "+v"(su[1]));
  {
    f32x4 sv[2];
#pragma unroll
    for (int hf = 0; hf < 2; hf++) {
      const int e = li & 3;
      const float s2v = e == 0 ? sq[hf][0] : (e == 1 ? sq[hf][1] : (e == 2 ? sq[hf][2] : sq[hf][3]));
      const float mean = su[hf][0] * (1.f / D_);
      const float var = fmaxf(s2v * (1.f / D_) - mean * mean, 0.f);
      const float sd = rbf(__builtin_amdgcn_sqrtf(rbf(rbf(var) + 1e-5f)));
      sv[hf] = f32x4{-rbf(mean), -1.44269504088896341f * __builtin_amdgcn_rcpf(sd), sd * sd, 0.f};
    }
    if ((li >> 2) == lg) { *(LAS f32x4*)(stp + (w * 16 + li) * 16) = sv[0]; *(LAS f32x4*)(stp + 1024 + (w * 16 + li) * 16) = sv[1]; }
  }
  BARRIER();
  {
    f32x4 sv[2][4];
#pragma unroll
    for (int hf = 0; hf < 2; hf++)
#pragma unroll
      for (int m = 0; m < 4; m++) sv[hf][m] = *(LAS f32x4*)(stp + hf * 1024 + ((m ^ w) * 16 + li) * 16);
    SCHED();
#pragma unroll
    for (int hf = 0; hf < 2; hf++) {
#pragma unroll
      for (int t = 0; t < 3; t++) {
        u32 hv[4];
#pragma unroll
        for (int m = 0; m < 4; m++) {
          const f32x4 s4 = sv[hf][m];
          const f32x2 ga = __builtin_elementwise_fma(cs[t].xy, s4.xx, acc1[hf][m][t].xy);
          const f32x2 ua = __builtin_elementwise_fma(cs[t].zw, s4.xx, acc1[hf][m][t].zw);
          const f32x2 ar = ga * s4.yy;
          const f32x2 ex = {__builtin_amdgcn_exp2f(ar.x), __builtin_amdgcn_exp2f(ar.y)};
          const f32x2 dn = __builtin_elementwise_fma(ex, s4.zz, s4.zz);
          const f32x2 rc = {__builtin_amdgcn_rcpf(dn.x), __builtin_amdgcn_rcpf(dn.y)};
          const f32x2 p = (ga * ua) * rc;
          hv[m] = pkbf(p.x, p.y);
        }
        swap32(hv[0], hv[2]); swap32(hv[1], hv[3]);
        swap16(hv[0], hv[1]); swap16(hv[2], hv[3]);
        __builtin_amdgcn_raw_buffer_store_b128(u32x4{hv[0], hv[1], hv[2], hv[3]}, rws, ((lg ^ w) * 48 * 1024) + (w * 16 + li) * 16 +
                                               WS_HID + ((8 * gr + 4 * hf) * 48 + 3 * gj + t) * 1024, 0, SC1);
      }
    }
  }
  STAMP_AT(23);
  VMWAIT(0);
  if (lane == 0) __hip_atomic_fetch_add((GAS u32*)(ws + WS_CTRL + (64 + gr * 4 + (gj >> 2)) * 256), 1u, RLX_AGENT);
  STAMP_AT(10);
  int hoff[4];
#pragma unroll
  for (int m = 0; m < 4; m++) { const int q = m ^ w, mf = 8 * gr + 4 * (q >> 1) + 2 * sub + (q & 1); hoff[m] = WS_HID + (mf * 48 + 12 * w) * 1024; }
  LAS char* red[4];
#pragma unroll
  for (int m = 1; m < 4; m++) red[m] = opaque(S + 81920 + lane * 16 + ((m ^ w) * 3 + (m - 1)) * 5120);
  red[0] = opaque(S + 81920 + lane * 16 + w * 15360);
  for (u32 spins = 0;; spins++) {
    const u32 f = poll32(ws + WS_CTRL + (64 + gr * 4 + w) * 256);
    if (__all(f >= 16u) || spins > SPIN_CAP) break;
  }
  asm volatile("" ::: "memory");
  STAMP_AT(11);
  f32x4 acc2[4][5];
  {
    u32x4 Hf[12][4];
    constexpr int PRE = 9;
    sfor<0, PRE * 4>([&](auto I) { constexpr int ks = I / 4, m = I % 4; Hf[ks][m] = __builtin_amdgcn_raw_buffer_load_b128(rws, voff + (ks & 3) * 1024, hoff[m] + (ks >> 2) * 4096, SC1); SCHED(); });
    sfor<0, 9>([&](auto K_) {
      constexpr int ks = K_;
      sfor<0, 4>([&](auto M_) {
        constexpr int m = M_;
        sfor<0, 5>([&](auto T_) { constexpr int t = T_; mf_bfa<ks == 0>(acc2[m][t], V[ks * 5 + t], Hf[ks][m]); });
        if constexpr (ks + PRE < 12) Hf[ks + PRE][m] = __builtin_amdgcn_raw_buffer_load_b128(rws, voff + ((ks + PRE) & 3) * 1024, hoff[m] + ((ks + PRE) >> 2) * 4096, SC1);
        SCHED();
      });
    });
    sfor<0, 4>([&](auto Q_) {
      constexpr int q = Q_, m = (q + 1) & 3;
      sfor<0, 15>([&](auto N_) {
        constexpr int n = N_, ks = 9 + n / 5, t = n % 5;
        mf_bfa<false>(acc2[m][t], V[ks * 5 + t], Hf[ks][m]);
        if constexpr (q > 0 && n >= 4 && n < 14 && (n & 1) == 0) { constexpr int tt = (n - 4) / 2; *(LAS f32x4*)(red[q] + tt * 1024) = acc2[q][tt]; }
        SCHED();
      });
    });
  }
  STAMP_AT(12);
  {
    BARRIER();
    f32x4 pv[5][3];
#pragma unroll
    for (int t = 0; t < 5; t++)
#pragma unroll
      for (int v = 0; v < 3; v++) pv[t][v] = *(LAS f32x4*)(red[0] + t * 1024 + v * 5120);
    asm volatile("s_nop 1" : "+v"(acc2[0][0]), "+v"(acc2[0][1]), "+v"(acc2[0][2]), "+v"(acc2[0][3]), "+v"(acc2[0][4]));
    u32 o[5][2];
#pragma unroll
    for (int t = 0; t < 5; t++) {
      const f32x4 rv = {bflo(res[t].x), bfhi(res[t].x), bflo(res[t].y), bfhi(res[t].y)};
      const f32x4 v = ((acc2[0][t] + rv) + pv[t][0]) + (pv[t][1] + pv[t][2]);
      o[t][0] = pkbf(v[0], v[1]);
      o[t][1] = pkbf(v[2], v[3]);
    }
    const int l = 128 * gr + 64 * (w >> 1) + 16 * (2 * sub + (w & 1)) + li;
    u16* const op = out + l * D_ + 80 * j2;
    *(u32x4*)(op + 8 * lg) = u32x4{o[0][0], o[0][1], o[1][0], o[1][1]};
    *(u32x4*)(op + 32 + 8 * lg) = u32x4{o[2][0], o[2][1], o[3][0], o[3][1]};
    *(u32x2*)(op + 64 + 4 * lg) = u32x2{o[4][0], o[4][1]};
  }
  STAMP_AT(13);
}

extern "C" void kernel_launch(void* const* d_in, const int* in_sizes, int n_in, void* d_out, int out_size, void* d_ws, size_t ws_size,
                              hipStream_t stream) {
  (void)hipFuncSetAttribute((const void*)fused_kernel, hipFuncAttributeMaxDynamicSharedMemorySize, LDS_BYTES);
  fused_kernel<<<256, 256, LDS_BYTES, stream>>>((const u16*)d_in[0], (const u16*)d_in[1], (const u16*)d_in[2], (const u16*)d_in[3], (const u16*)d_in[4],
      (const u16*)d_in[5], (const u16*)d_in[6], (const u16*)d_in[8], (const u16*)d_in[9], (const u16*)d_in[10], (u16*)d_out, (char*)d_ws);
}
```

```cpp
#define PROBE_A 99
#define PROBE_B 0
#include <hip/hip_runtime.h>
#include <stdint.h>

typedef unsigned short u16;
typedef unsigned u32;
typedef unsigned long long u64;
typedef u32 u32x2 __attribute__((ext_vector_type(2)));
typedef u32 u32x4 __attribute__((ext_vector_type(4)));
typedef float f32x2 __attribute__((ext_vector_type(2)));
typedef float f32x4 __attribute__((ext_vector_type(4)));
typedef float f32x16 __attribute__((ext_vector_type(16)));
typedef _Float16 f16x2 __attribute__((ext_vector_type(2)));
typedef __bf16 bf16x2 __attribute__((ext_vector_type(2)));
typedef short s16x4 __attribute__((ext_vector_type(4)));
#define LAS __attribute__((address_space(3)))
#define GAS __attribute__((address_space(1)))
#define DI __device__ __forceinline__
#define RLX_AGENT __ATOMIC_RELAXED, __HIP_MEMORY_SCOPE_AGENT
typedef __amdgpu_buffer_rsrc_t rsrc_t;

constexpr int L_ = 2048, D_ = 640, N_ = 192, F_ = 1536;
constexpr int SC1 = 16;
constexpr int CODE_PREFETCH_BYTES = 36864;
constexpr u32 SPIN_CAP = 1u << 18;

constexpr u32 WS_CTRL = 0;
constexpr u32 WS_EPOCH = 0x10000;
constexpr u32 WS_TFLAG = 0x11000;
constexpr u32 WS_XFLAG = 0x18000;
constexpr u32 WS_ST1 = 0x20000;
constexpr u32 WS_W13 = 0x100000;
constexpr u32 WS_W2 = 0x500000;
constexpr u32 WS_HT = 0x700000;
constexpr u32 WS_HID = 0xA00000;
constexpr u32 WS_DBG = 0x1000000;
constexpr u32 WS_XT = 0x1100000;
constexpr u32 WS_BYTES = 0x1400000;

constexpr int TROW = 2352;
constexpr int IMG_CP = 4160, IMG_SZ = 2 * IMG_CP;
constexpr int LD_PX = 3 * IMG_SZ;
constexpr int LD_U = 124800;
constexpr int U_SZ = 2368;
constexpr int LD_XT = LD_U + 6 * U_SZ;
constexpr int LD_KC = LD_XT + 3 * 4096;
constexpr int KC_SZ = 32 * 56;
constexpr int LD_MISC = LD_KC + 3 * KC_SZ;
constexpr int LD_TS = LD_U;
constexpr int LDS_BYTES = 163840;
static_assert(53 * TROW <= LD_U && LD_PX + 98304 <= LD_U, "");
static_assert(LD_TS + 10240 + 2 * 4224 + 5120 <= LD_KC, "");

template <int I> struct IC { static constexpr int value = I; constexpr operator int() const { return I; } };
template <int B, int E, class F> DI void sfor(F&& f) { if constexpr (B < E) { f(IC<B>{}); sfor<B + 1, E>(f); } }
template <int I> DI void ldw(u32x4& dst, u32 voff, const char* base) {
  asm volatile("global_load_dwordx4 %0, %1, %2 offset:%3 sc1" : "=a"(dst) : "v"(voff), "s"(base + (I >> 2) * 4096), "i"((I & 3) * 1024));
}
#define LDW(dst, voff, base, i) ldw<i>(dst, voff, base)
DI float asf(u32 u) { return __builtin_bit_cast(float, u); }
DI u32 asu(float f) { return __builtin_bit_cast(u32, f); }
DI float bflo(u32 u) { return asf(u << 16); }
DI float bfhi(u32 u) { return asf(u & 0xffff0000u); }
DI u32 pkbf(float a, float b) { f32x2 v = {a, b}; bf16x2 h = __builtin_convertvector(v, bf16x2); return __builtin_bit_cast(u32, h); }
DI u32 pkh(float a, float b) { f32x2 v = {a, b}; f16x2 h = __builtin_convertvector(v, f16x2); return __builtin_bit_cast(u32, h); }
DI u32 pkh2(f32x2 v) { f16x2 h = __builtin_convertvector(v, f16x2); return __builtin_bit_cast(u32, h); }
DI float rbf(float a) { return bflo(pkbf(a, a)); }
template <int CTRL> DI float dpp_add(float v) { return v + asf(__builtin_amdgcn_update_dpp(0, asu(v), CTRL, 0xf, 0xf, true)); }
template <int CTRL> DI u32 dpp_max(u32 v) { u32 o = __builtin_amdgcn_update_dpp(0, v, CTRL, 0xf, 0xf, true); return v > o ? v : o; }
DI f32x2 cmul(f32x2 a, f32x2 b, f32x2 bs) { f32x2 t = a.xx * b; return __builtin_elementwise_fma(a.yy, bs, t); }
DI u32x4 trfrag(LAS char* p0, LAS char* p1) {
  u32x2 lo = __builtin_bit_cast(u32x2, __builtin_amdgcn_ds_read_tr16_b64_v4i16((LAS s16x4*)p0));
  u32x2 hi = __builtin_bit_cast(u32x2, __builtin_amdgcn_ds_read_tr16_b64_v4i16((LAS s16x4*)p1));
  return u32x4{lo.x, lo.y, hi.x, hi.y};
}
DI u32x2 tr64(LAS char* p) { return __builtin_bit_cast(u32x2, __builtin_amdgcn_ds_read_tr16_b64_v4i16((LAS s16x4*)p)); }
DI void swap16(u32& a, u32& b) { auto r = __builtin_amdgcn_permlane16_swap(a, b, false, false); a = r[0]; b = r[1]; }
DI void swap32(u32& a, u32& b) { auto r = __builtin_amdgcn_permlane32_swap(a, b, false, false); a = r[0]; b = r[1]; }
#define VMWAIT(n) asm volatile("s_waitcnt vmcnt(%0)" ::"i"(n) : "memory")
#define BARRIER() do { asm volatile("s_waitcnt lgkmcnt(0)" ::: "memory"); __builtin_amdgcn_s_barrier(); asm volatile("" ::: "memory"); } while (0)
#define SCHED() __builtin_amdgcn_sched_barrier(0)

#define DEF_MFMA(name, op, T, CA) \
  template <bool Z> DI void name(T& acc, const u32x4& a, const u32x4& b) { \
    if constexpr (Z) asm volatile(op " %0, %1, %2, 0" : "=&v"(acc) : CA(a), "v"(b)); \
    else asm volatile(op " %0, %1, %2, %0" : "+v"(acc) : CA(a), "v"(b)); }
DEF_MFMA(mf_bfa, "v_mfma_f32_16x16x32_bf16", f32x4, "a")
DEF_MFMA(mf_bfv, "v_mfma_f32_16x16x32_bf16", f32x4, "v")
DEF_MFMA(mf_hv, "v_mfma_f32_16x16x32_f16", f32x4, "v")
DEF_MFMA(mf_h32, "v_mfma_f32_32x32x16_f16", f32x16, "v")
DI u32 poll32(const char* p) { return __hip_atomic_load((GAS u32*)p, RLX_AGENT); }
template <int N> DI void vmwait() { asm volatile("s_waitcnt vmcnt(%0)" ::"i"(N) : "memory"); }
template <int IMM> DI void set_m0(u32 base) { asm volatile("s_add_u32 m0, %0, %1" ::"s"(base), "i"(IMM) : "scc"); }
template <int IMM> DI void set_m0n(u32 base) { asm volatile("s_add_u32 m0, %0, %1\n\ts_nop 0" ::"s"(base), "i"(IMM) : "scc"); }
DI void dma_m0(u32 voff, rsrc_t r) { asm volatile("buffer_load_dwordx4 %0, %1, m0 offen sc1 lds" ::"v"(voff), "s"(r) : "memory"); }
template <int N> DI void lgkwait() { __builtin_amdgcn_s_waitcnt(0xC07F | (N << 8)); }
template <int O0, int O1, int O2, int O3> DI void put12(const u32 (&v)[4][3]) {
  asm volatile("ds_write_addtid_b32 %0 offset:%12\n\tds_write_addtid_b32 %1 offset:%12+256\n\tds_write_addtid_b32 %2 offset:%12+512\n\t"
               "ds_write_addtid_b32 %3 offset:%13\n\tds_write_addtid_b32 %4 offset:%13+256\n\tds_write_addtid_b32 %5 offset:%13+512\n\t"
               "ds_write_addtid_b32 %6 offset:%14\n\tds_write_addtid_b32 %7 offset:%14+256\n\tds_write_addtid_b32 %8 offset:%14+512\n\t"
               "ds_write_addtid_b32 %9 offset:%15\n\tds_write_addtid_b32 %10 offset:%15+256\n\tds_write_addtid_b32 %11 offset:%15+512"
               ::"v"(v[0][0]), "v"(v[0][1]), "v"(v[0][2]), "v"(v[1][0]), "v"(v[1][1]), "v"(v[1][2]), "v"(v[2][0]), "v"(v[2][1]), "v"(v[2][2]),
                 "v"(v[3][0]), "v"(v[3][1]), "v"(v[3][2]), "i"(O0), "i"(O1), "i"(O2), "i"(O3) : "memory");
}
template <int O0> DI void put3(const u32 (&v)[3]) {
  asm volatile("ds_write_addtid_b32 %0 offset:%3\n\tds_write_addtid_b32 %1 offset:%3+256\n\tds_write_addtid_b32 %2 offset:%3+512"
               ::"v"(v[0]), "v"(v[1]), "v"(v[2]), "i"(O0) : "memory");
}
template <class T> DI T opaque(T v) { asm volatile("" : "+v"(v)); return v; }

#define PIN(v) asm volatile("" ::"v"(v))
#define STAMP_AT(i) do { if ((i) == PROBE_A) { SCHED(); pta = __builtin_amdgcn_s_memrealtime(); SCHED(); } if ((i) == PROBE_B) { SCHED(); ptb = __builtin_amdgcn_s_memrealtime(); SCHED(); } } while (0)
#define STAMP_W(i, t) do {} while (0)

DI f32x2 cexp(float ap, float bp) {
  const float e = __builtin_amdgcn_exp2f(ap * 1.44269504088896341f);
  const float C_HI = 0.15915494309189535f, C_LO = 6.4206382e-9f;
  const float x = bp * C_HI;
  const float n = __builtin_rintf(x);
  const float f = __builtin_fmaf(bp, C_LO, __builtin_fmaf(bp, C_HI, -n));
  return f32x2{e * __builtin_amdgcn_cosf(f), e * __builtin_amdgcn_sinf(f)};
}

DI u32 kc_index(int s) {
  const int p = (int)rbf((float)s);
  const u32 q = (u32)(p - 1);
  const u32 j = (q >> 3) & 31, sg = (q >> 8) & 7, c = q & 7;
  const u32 base = __builtin_amdgcn_perm(0x13121110u, 0x0E0C0800u, sg | 0x0c0c0c00u);
  const u32 sh = (0x33332210u >> (4 * sg)) & 3;
  u32 off = j * 56 + (base + (c >> sh)) * 2;
  off = s == 0 ? 40u : off;
  off = s < 0 ? 42u : off;
  return off;
}
DI float fexp(float x) { return __builtin_amdgcn_exp2f(x * 1.44269504088896341f); }

#define MAPL 1
constexpr int NW_CONV = 36;

__global__ __launch_bounds__(256, 1) void fused_kernel(const u16* __restrict__ x, const u16* __restrict__ lar, const u16* __restrict__ aim,
    const u16* __restrict__ cre, const u16* __restrict__ cim, const u16* __restrict__ ldt, const u16* __restrict__ dsk,
    const u16* __restrict__ w1, const u16* __restrict__ w3, const u16* __restrict__ w2, u16* __restrict__ out, char* __restrict__ ws) {
  extern __shared__ __attribute__((aligned(16))) char smem[];
  LAS char* const S = (LAS char*)smem;
  u64 pta = 0, ptb = 0;
  if (PROBE_A == 99) pta = __builtin_amdgcn_s_memrealtime();
  u64 pc;
  asm volatile("s_getpc_b64 %0" : "=s"(pc));
  asm volatile("" ::"s"(x), "s"(lar), "s"(aim), "s"(cre), "s"(cim), "s"(ldt), "s"(dsk), "s"(w1), "s"(w3), "s"(w2), "s"(out), "s"(ws));
  const int tid = threadIdx.x, lane = tid & 63, w = __builtin_amdgcn_readfirstlane(tid >> 6);
  const int b = blockIdx.x, xcd = b & 7, slot = b >> 3;
  const int li = lane & 15, lg = lane >> 4;
  const rsrc_t rws = __builtin_amdgcn_make_buffer_rsrc(ws, 0, WS_BYTES, 0x00020000);
  const rsrc_t rx = __builtin_amdgcn_make_buffer_rsrc((void*)x, 0, L_ * D_ * 2, 0x00020000);
  STAMP_AT(0);

  const int pr = xcd * 16 + (slot >> 1), odd = slot & 1;
  const int ch0 = 5 * pr + (odd ? 3 : 0), nch = odd ? 2 : 3;
  const int gr = (xcd >> MAPL) * (2 << MAPL) + (slot >> (4 - MAPL)), gj = (xcd & ((1 << MAPL) - 1)) * (16 >> MAPL) + (slot & ((16 >> MAPL) - 1));
  const int j2 = gj >> 1, sub = gj & 1;

  u32 tag, gidx[9];
  if (w < 3) {
    const int ch = ch0 + (w < nch ? w : 0);
    const rsrc_t r0 = __builtin_amdgcn_make_buffer_rsrc((void*)(lar + ch * N_), 0, N_ * 2, 0x00020000);
    const rsrc_t r1 = __builtin_amdgcn_make_buffer_rsrc((void*)(aim + ch * N_), 0, N_ * 2, 0x00020000);
    const rsrc_t r2 = __builtin_amdgcn_make_buffer_rsrc((void*)(cre + ch * N_), 0, N_ * 2, 0x00020000);
    const rsrc_t r3 = __builtin_amdgcn_make_buffer_rsrc((void*)(cim + ch * N_), 0, N_ * 2, 0x00020000);
    const rsrc_t r4 = __builtin_amdgcn_make_buffer_rsrc((void*)ldt, 0, D_ * 2, 0x00020000);
    const rsrc_t r5 = __builtin_amdgcn_make_buffer_rsrc((void*)dsk, 0, D_ * 2, 0x00020000);
    const u32 ldtv = __builtin_amdgcn_raw_buffer_load_b16(r4, ch * 2, 0, 0);
    const u32 la2 = __builtin_amdgcn_raw_buffer_load_b32(r0, lane * 4, 0, 0), la1 = __builtin_amdgcn_raw_buffer_load_b16(r0, 256 + lane * 2, 0, 0);
    const u32 ai2 = __builtin_amdgcn_raw_buffer_load_b32(r1, lane * 4, 0, 0), ai1 = __builtin_amdgcn_raw_buffer_load_b16(r1, 256 + lane * 2, 0, 0);
    const u32 cr2 = __builtin_amdgcn_raw_buffer_load_b32(r2, lane * 4, 0, 0), cr1 = __builtin_amdgcn_raw_buffer_load_b16(r2, 256 + lane * 2, 0, 0);
    const u32 ci2 = __builtin_amdgcn_raw_buffer_load_b32(r3, lane * 4, 0, 0), ci1 = __builtin_amdgcn_raw_buffer_load_b16(r3, 256 + lane * 2, 0, 0);
    const u32 dsv = __builtin_amdgcn_raw_buffer_load_b16(r5, ch * 2, 0, 0);
    tag = __builtin_amdgcn_raw_buffer_load_b32(rws, WS_EPOCH, 0, SC1);
    const rsrc_t rc = __builtin_amdgcn_make_buffer_rsrc((void*)(pc & ~255ull), 0, CODE_PREFETCH_BYTES, 0x00020000);
    const u32 cpf = __builtin_amdgcn_raw_buffer_load_b32(rc, (((slot & 1) * 3 + w) * 64 + lane) * 128, 0, 0);
    asm volatile("" ::: "memory");
    SCHED();
#pragma unroll
    for (int k = 0; k < 9; k++) gidx[k] = opaque(kc_index(2047 - 8 * tid - k));
    SCHED();
    STAMP_AT(26);
    PIN(ldtv); PIN(la2); PIN(la1); PIN(ai2); PIN(ai1);
    STAMP_AT(27);
    const float dt = rbf(fexp(bflo(ldtv)));
    f32x2 w1v[3], w8v[3], w256v[3], cc[3];
#pragma unroll
    for (int m = 0; m < 3; m++) {
      const float la = m == 0 ? bflo(la2) : (m == 1 ? bfhi(la2) : bflo(la1));
      const float ai = m == 0 ? bflo(ai2) : (m == 1 ? bfhi(ai2) : bflo(ai1));
      const float a = -rbf(fexp(la)) * dt, bb = ai * dt;
      w8v[m] = cexp(8.f * a, 8.f * bb);
      w1v[m] = cexp(a, -bb);
      w256v[m] = cexp(256.f * a, -256.f * bb);
    }
    PIN(w8v[0]); PIN(w8v[1]); PIN(w8v[2]); PIN(w1v[0]); PIN(w1v[1]); PIN(w1v[2]); PIN(w256v[0]); PIN(w256v[1]); PIN(w256v[2]);
    STAMP_AT(14);
    const u32 m0lo = w * 784;
    set_m0n<0>(m0lo);
    {
      f32x2 ws8[3], v[3];
      u32 pk[4][3];
#pragma unroll
      for (int m = 0; m < 3; m++) { ws8[m] = f32x2{-w8v[m].y, w8v[m].x}; v[m] = w8v[m]; pk[0][m] = 0x00003c00u; pk[1][m] = pkh2(v[m]); }
      sfor<0, 8>([&](auto G_) {
        constexpr int g = G_;
#pragma unroll
        for (int i = (g == 0 ? 2 : 0); i < 4; i++)
#pragma unroll
          for (int m = 0; m < 3; m++) { v[m] = cmul(v[m], w8v[m], ws8[m]); pk[i][m] = pkh2(v[m]); }
        if constexpr (g == 6) set_m0n<24 * TROW>(m0lo);
        constexpr int r0_ = g < 6 ? 4 * g : 4 * g - 24;
        put12<r0_ * TROW, (r0_ + 1) * TROW, (r0_ + 2) * TROW, (r0_ + 3) * TROW>(pk);
      });
    }
    STAMP_AT(28);
    u32 cm = cr2 & 0x7fffu;
    { u32 t;
      t = (cr2 >> 16) & 0x7fffu; cm = cm > t ? cm : t;
      t = ci2 & 0x7fffu; cm = cm > t ? cm : t;
      t = (ci2 >> 16) & 0x7fffu; cm = cm > t ? cm : t;
      t = cr1 & 0x7fffu; cm = cm > t ? cm : t;
      t = ci1 & 0x7fffu; cm = cm > t ? cm : t; }
    cm = dpp_max<0xB1>(cm); cm = dpp_max<0x4E>(cm); cm = dpp_max<0x141>(cm); cm = dpp_max<0x140>(cm);
    { u32 a0 = __builtin_amdgcn_readlane(cm, 0), a1 = __builtin_amdgcn_readlane(cm, 16), a2 = __builtin_amdgcn_readlane(cm, 32), a3 = __builtin_amdgcn_readlane(cm, 48);
      a0 = a0 > a1 ? a0 : a1; a2 = a2 > a3 ? a2 : a3; cm = a0 > a2 ? a0 : a2; }
    int ec = (int)(cm >> 7);
    ec = ec < 16 ? 16 : (ec > 254 ? 254 : ec);
    const float s1 = asf((u32)(127 + 13 + 127 - ec) << 23);
    const float s1i = asf((u32)(ec - 13) << 23);
    const float bound = 544.f * dt * bflo(cm) + fabsf(bflo(dsv));
    int eb = (int)(asu(bound) >> 23);
    eb = eb < 16 ? 16 : (eb > 254 ? 254 : eb);
    const float s2 = asf((u32)(127 + 14 + 127 - eb) << 23);
    const float s2i = asf((u32)(eb - 14) << 23);
    cc[0] = f32x2{bflo(cr2) * s1, -bflo(ci2) * s1}; cc[1] = f32x2{bfhi(cr2) * s1, -bfhi(ci2) * s1}; cc[2] = f32x2{bflo(cr1) * s1, -bflo(ci1) * s1};
    set_m0n<27 * TROW>(m0lo);
    {
      f32x2 ws1[3], ws256[3], v[3], k2[3], k4[3], k6[3];
      u32 pk[4][3];
#pragma unroll
      for (int m = 0; m < 3; m++) { ws1[m] = f32x2{-w1v[m].y, w1v[m].x}; ws256[m] = f32x2{-w256v[m].y, w256v[m].x}; v[m] = cc[m]; pk[0][m] = pkh2(v[m]); }
      put3<25 * TROW>(pk[0]);
#pragma unroll
      for (int i = 0; i < 4; i++)
#pragma unroll
        for (int m = 0; m < 3; m++) { v[m] = cmul(v[m], w1v[m], ws1[m]); pk[i][m] = pkh2(v[m]); if (i == 1) k2[m] = v[m]; if (i == 3) k4[m] = v[m]; }
      put12<5 * TROW, 6 * TROW, 7 * TROW, 8 * TROW>(pk);
#pragma unroll
      for (int i = 0; i < 4; i++)
#pragma unroll
        for (int m = 0; m < 3; m++) { v[m] = cmul(v[m], w1v[m], ws1[m]); pk[i][m] = pkh2(v[m]); if (i == 1) k6[m] = v[m]; }
      put12<9 * TROW, 10 * TROW, 11 * TROW, 12 * TROW>(pk);
#pragma unroll
      for (int m = 0; m < 3; m++) {
        k2[m] = cmul(k2[m], w256v[m], ws256[m]); pk[0][m] = pkh2(k2[m]);
        k4[m] = cmul(k4[m], w256v[m], ws256[m]); pk[1][m] = pkh2(k4[m]);
        k6[m] = cmul(k6[m], w256v[m], ws256[m]); pk[2][m] = pkh2(k6[m]);
        v[m] = cmul(v[m], w256v[m], ws256[m]); pk[3][m] = pkh2(v[m]);
      }
      put12<13 * TROW, 14 * TROW, 15 * TROW, 16 * TROW>(pk);
#pragma unroll
      for (int i = 0; i < 2; i++)
#pragma unroll
        for (int m = 0; m < 3; m++) {
          k4[m] = cmul(k4[m], w256v[m], ws256[m]); pk[2 * i][m] = pkh2(k4[m]);
          v[m] = cmul(v[m], w256v[m], ws256[m]); pk[2 * i + 1][m] = pkh2(v[m]);
        }
      put12<17 * TROW, 18 * TROW, 19 * TROW, 20 * TROW>(pk);
#pragma unroll
      for (int i = 0; i < 4; i++)
#pragma unroll
        for (int m = 0; m < 3; m++) { v[m] = cmul(v[m], w256v[m], ws256[m]); pk[i][m] = pkh2(v[m]); }
      put12<21 * TROW, 22 * TROW, 23 * TROW, 24 * TROW>(pk);
    }
    STAMP_AT(15);
    const int rr = lane & 31, hf = lane >> 5;
    LAS char* const pa = S + (32 + (rr > 20 ? 20 : rr)) * TROW + w * 784 + hf * 16;
    LAS char* const pb = S + rr * TROW + w * 784 + hf * 16;
    f32x16 kacc;
    u32x4 fa[24], fb[24];
    sfor<0, 28>([&](auto K_) {
      constexpr int ks = K_;
      if constexpr (ks < 24) { fa[ks] = *(LAS u32x4*)(pa + ks * 32); fb[ks] = *(LAS u32x4*)(pb + ks * 32); }
      if constexpr (ks >= 4) mf_h32<ks == 4>(kacc, fa[ks - 4], fb[ks - 4]);
      SCHED();
    });
    asm volatile("s_nop 7\n\ts_nop 7" : "+v"(kacc));
    const float fac = 2.f * dt * s1i * s2;
    LAS char* const kc = S + LD_KC + w * KC_SZ + rr * 56 + hf * 8;
    const float k8 = kacc[8] * fac + (hf ? bflo(dsv) * s2 : 0.f);
    const u32x2 q0 = {pkh(kacc[0] * fac, kacc[1] * fac), pkh(kacc[2] * fac, kacc[3] * fac)};
    const u32x2 q1 = {pkh(kacc[4] * fac, kacc[5] * fac), pkh(kacc[6] * fac, kacc[7] * fac)};
    const u32x2 q2 = {pkh(k8, hf ? 0.f : kacc[9] * fac), pkh(kacc[10] * fac, kacc[11] * fac)};
    *(LAS u32x2*)(kc) = q0; *(LAS u32x2*)(kc + 16) = q1; *(LAS u32x2*)(kc + 32) = q2;
    *(LAS float*)(S + LD_MISC + w * 4) = s2i;
    asm volatile("" ::"v"(cpf));
  } else {
    __builtin_amdgcn_raw_buffer_store_b32(0u, rws, WS_CTRL + b * 256 + lane * 4, 0, SC1);
    u32x4 xt[10], xr[10];
    int vo10[5];
#pragma unroll
    for (int i = 0; i < 5; i++) { const int id = lane + 64 * i, rho = id / 10, c = id - 10 * rho; vo10[i] = (rho * D_ + 8 * c) * 2; }
#pragma unroll
    for (int i = 0; i < 10; i++) xt[i] = __builtin_amdgcn_raw_buffer_load_b128(rx, vo10[i % 5], ((64 * slot + 32 * (i / 5)) * D_ + 80 * xcd) * 2, 0);
    const int row = 8 * b + (lane >> 3), part = lane & 7;
#pragma unroll
    for (int i = 0; i < 10; i++) xr[i] = __builtin_amdgcn_raw_buffer_load_b128(rx, row * (D_ * 2) + (part + 8 * i) * 16, 0, 0);
    tag = __builtin_amdgcn_raw_buffer_load_b32(rws, WS_EPOCH, 0, SC1);
    asm volatile("" ::: "memory");
    SCHED();
    const rsrc_t rw1 = __builtin_amdgcn_make_buffer_rsrc((void*)w1, 0, D_ * F_ * 2, 0x00020000);
    const rsrc_t rw3 = __builtin_amdgcn_make_buffer_rsrc((void*)w3, 0, D_ * F_ * 2, 0x00020000);
    const rsrc_t rw2 = __builtin_amdgcn_make_buffer_rsrc((void*)w2, 0, D_ * F_ * 2, 0x00020000);
    u32x4 wa[4][4], wb[2][5];
    const bool do13 = gr < 15;
    const int vo13 = ((lane >> 2) * F_ + 8 * (lane & 3)) * 2;
#pragma unroll
    for (int u = 0; u < 4; u++) {
      const int su = do13 ? 4 * gr + u : u, ks = su / 3, t = su - 3 * ks;
      const int so = (32 * ks * F_ + 96 * gj + 32 * t) * 2;
      wa[u][0] = __builtin_amdgcn_raw_buffer_load_b128(rw1, vo13, so, 0); wa[u][1] = __builtin_amdgcn_raw_buffer_load_b128(rw3, vo13, so, 0);
      wa[u][2] = __builtin_amdgcn_raw_buffer_load_b128(rw1, vo13, so + 32 * F_, 0); wa[u][3] = __builtin_amdgcn_raw_buffer_load_b128(rw3, vo13, so + 32 * F_, 0);
    }
#pragma unroll
    for (int i = 0; i < 5; i++) {
#pragma unroll
      for (int u = 0; u < 2; u++) {
        const int un = (u == 1 && b < 128) ? b + 256 : b, uj = un & 7, uk = un >> 3;
        wb[u][i] = __builtin_amdgcn_raw_buffer_load_b128(rw2, vo10[i], (32 * uk * D_ + 80 * uj) * 2, 0);
      }
    }
    asm volatile("" ::: "memory");
    SCHED();
#pragma unroll
    for (int k = 0; k < 9; k++) gidx[k] = opaque(kc_index(2047 - 8 * tid - k));
    SCHED();
    STAMP_W(29, 192);
    PIN(xt[9]);
    STAMP_W(30, 192);
    {
      LAS char* const XS = S + LD_TS;
#pragma unroll
      for (int i = 0; i < 10; i++) *(LAS u32x4*)(XS + (lane + 64 * i) * 16) = xt[i];
      LAS char* const p0 = XS + (8 * (4 * (lg & 1) + (lane & 3)) + ((lane >> 2) & 3)) * 160 + (lg >> 1) * 8;
      u32x4 fr[10];
#pragma unroll
      for (int k = 0; k < 10; k++) fr[k] = trfrag(p0 + k * 16, p0 + 640 + k * 16);
      const int xo = WS_XT + (20 * xcd + (lg >> 1)) * 16384 + slot * 512 + (lane & 31) * 16;
#pragma unroll
      for (int k = 0; k < 10; k++) __builtin_amdgcn_raw_buffer_store_b128(fr[k], rws, xo + k * 32768, 0, SC1);
      asm volatile("" ::: "memory");
      SCHED();
    }
    STAMP_W(24, 192);
    f32x2 xv[40], sm2 = {0.f, 0.f};
#pragma unroll
    for (int i = 0; i < 10; i++)
#pragma unroll
      for (int e = 0; e < 4; e++) { xv[4 * i + e] = f32x2{bflo(xr[i][e]), bfhi(xr[i][e])}; sm2 += xv[4 * i + e]; }
    float sm = sm2.x + sm2.y;
    sm = dpp_add<0xB1>(sm); sm = dpp_add<0x4E>(sm); sm = dpp_add<0x141>(sm);
    const float mean = sm * (1.f / D_);
    f32x2 q2 = {0.f, 0.f};
#pragma unroll
    for (int i = 0; i < 40; i++) { const f32x2 d = xv[i] - mean; q2 = __builtin_elementwise_fma(d, d, q2); }
    float q = q2.x + q2.y;
    q = dpp_add<0xB1>(q); q = dpp_add<0x4E>(q); q = dpp_add<0x141>(q);
    const float sd = __builtin_amdgcn_sqrtf(rbf(rbf(q * (1.f / D_)) + 1e-5f));
    if (part == 0) __builtin_amdgcn_raw_buffer_store_b64(u32x2{pkbf(mean, sd), tag + 1u}, rws, WS_ST1 + row * 8, 0, SC1);
    asm volatile("" ::: "memory");
    STAMP_W(31, 192);
    VMWAIT(1);
    if (lane == 0) __builtin_amdgcn_raw_buffer_store_b32(tag + 1u, rws, WS_XFLAG + (xcd * 32 + slot) * 128, 0, SC1);
    STAMP_W(16, 192);
    LAS char* TS = S + LD_TS + 10240;
    if (do13) {
#pragma unroll
      for (int u = 0; u < 4; u++) {
        const int su = 4 * gr + u, ks = su / 3, t = su - 3 * ks;
        TS = S + LD_TS + 10240 + (u & 1) * 4224;
        LAS char* const pw = TS + (lane & 3) * 1056 + (lane >> 2) * 32;
#pragma unroll
        for (int hh = 0; hh < 2; hh++) {
          const u32x4 g = wa[u][2 * hh], up = wa[u][2 * hh + 1];
          *(LAS u32x4*)(pw + hh * 512) = u32x4{g.x, up.x, g.y, up.y};
          *(LAS u32x4*)(pw + hh * 512 + 16) = u32x4{g.z, up.z, g.w, up.w};
        }
#pragma unroll
        for (int f = 0; f < 4; f++) {
          const u32x4 fr = trfrag(TS + f * 1056 + lane * 8, TS + f * 1056 + 512 + lane * 8);
          __builtin_amdgcn_raw_buffer_store_b128(fr, rws, lane * 16 + WS_W13 + (((gj * 4 + f) * 20 + ks) * 3 + t) * 1024, 0, SC1);
        }
      }
    }
#pragma unroll
    for (int u = 0; u < 2; u++) {
      if (u == 0 || b < 128) {
        const int un = u == 1 ? b + 256 : b, uj = un & 7, uk = un >> 3;
        TS = u == 0 ? S + LD_TS + 10240 + 2 * 4224 : S + LD_TS;
#pragma unroll
        for (int i = 0; i < 5; i++) *(LAS u32x4*)(TS + (lane + 64 * i) * 16) = wb[u][i];
        LAS char* const pr0 = TS + (8 * lg + ((lane >> 2) & 3)) * 160;
#pragma unroll
        for (int t = 0; t < 5; t++) {
          const int col = t < 4 ? 32 * (t >> 1) + 8 * (lane & 3) + 4 * (t & 1) : 64 + 4 * (lane & 3);
          const u32x4 fr = trfrag(pr0 + col * 2, pr0 + 640 + col * 2);
          __builtin_amdgcn_raw_buffer_store_b128(fr, rws, lane * 16 + WS_W2 + ((uj * 48 + uk) * 5 + t) * 1024, 0, SC1);
        }
      }
    }
    STAMP_W(17, 192);
    VMWAIT(0);
    if (lane == 0) __builtin_amdgcn_raw_buffer_store_b32(tag + 1u, rws, WS_TFLAG + gj * 256 + gr * 4, 0, SC1);
    STAMP_W(18, 192);
  }
  tag += 1u;
  STAMP_AT(1);
  u32x4 st[4];
#pragma unroll
  for (int i = 0; i < 4; i++) st[i] = __builtin_amdgcn_raw_buffer_load_b128(rws, WS_ST1 + tid * 64 + i * 16, 0, SC1);
  u32 xf = poll32(ws + WS_XFLAG + (xcd * 32 + w * 8 + (lane & 7)) * 128);
  BARRIER();
  STAMP_AT(2);
  for (u32 spins = 0;; spins++) {
    if (__all(xf == tag) || spins > SPIN_CAP) break;
    xf = poll32(ws + WS_XFLAG + (xcd * 32 + w * 8 + (lane & 7)) * 128);
  }
  asm volatile("" ::: "memory");
  u32x4 xc[3];
#pragma unroll
  for (int c = 0; c < 3; c++) { const int ch = ch0 + (c < nch ? c : 0); xc[c] = __builtin_amdgcn_raw_buffer_load_b128(rws, tid * 64, WS_XT + (ch >> 2) * 16384 + (ch & 3) * 16, SC1); }
  STAMP_AT(25);
  {
    u32 e[3][9];
#pragma unroll
    for (int c = 0; c < 3; c++)
#pragma unroll
      for (int k = 0; k < 9; k++) e[c][k] = *(LAS u16*)(S + LD_KC + c * KC_SZ + gidx[k]);
    SCHED();
    u32x4 p1[3], p0[3];
#pragma unroll
    for (int c = 0; c < 3; c++)
#pragma unroll
      for (int n = 0; n < 4; n++) { p1[c][n] = e[c][2 * n] | (e[c][2 * n + 1] << 16); p0[c][n] = e[c][2 * n + 1] | (e[c][2 * n + 2] << 16); }
    SCHED();
#pragma unroll
    for (int c = 0; c < 3; c++) { *(LAS u32x4*)(S + c * IMG_SZ + tid * 16) = p0[c]; *(LAS u32x4*)(S + c * IMG_SZ + IMG_CP + tid * 16) = p1[c]; }
    if (tid < 12) *(LAS u32x4*)(S + (tid >> 1) * IMG_CP + 4096 + (tid & 1) * 16) = u32x4{0, 0, 0, 0};
    if (tid >= 128 && tid < 224) *(LAS u32x4*)(S + LD_U + ((tid - 128) >> 4) * U_SZ + (tid & 15) * 16) = u32x4{0, 0, 0, 0};
  }
  STAMP_AT(3);
  for (u32 spins = 0;; spins++) {
    bool ok = true;
#pragma unroll
    for (int i = 0; i < 4; i++) ok = ok && st[i].y == tag && st[i].w == tag;
    if (__all(ok) || spins > SPIN_CAP) break;
    asm volatile("" ::: "memory");
#pragma unroll
    for (int i = 0; i < 4; i++) st[i] = __builtin_amdgcn_raw_buffer_load_b128(rws, WS_ST1 + tid * 64 + i * 16, 0, SC1);
  }
  u32 tf = poll32(ws + WS_TFLAG + gj * 256 + li * 4);
  STAMP_AT(4);
  {
    float ra[8], rb[8];
#pragma unroll
    for (int i = 0; i < 8; i++) { const u32 pl = (i & 1) ? st[i >> 1].z : st[i >> 1].x; ra[i] = __builtin_amdgcn_rcpf(bfhi(pl)); rb[i] = -bflo(pl) * ra[i]; }
    u32x4 v[3];
#pragma unroll
    for (int c = 0; c < 3; c++)
#pragma unroll
      for (int i = 0; i < 4; i++) {
        v[c][i] = pkh(fmaf(bflo(xc[c][i]), ra[2 * i], rb[2 * i]), fmaf(bfhi(xc[c][i]), ra[2 * i + 1], rb[2 * i + 1]));
      }
    SCHED();
#pragma unroll
    for (int c = 0; c < 3; c++) *(LAS u32x4*)(S + LD_U + (2 * c + (tid & 1)) * U_SZ + ((tid >> 1) + 16) * 16) = v[c];
#pragma unroll
    for (int c = 0; c < 3; c++) *(LAS u32x4*)(S + LD_XT + c * 4096 + tid * 16) = xc[c];
  }
  LAS char* hb[3][4];
  {
    LAS char* const h0 = S + (li & 1) * IMG_CP + 4092 - 32 * lg - 4 * (li >> 1) - 128 * w - 512;
#pragma unroll
    for (int c = 0; c < 3; c++)
#pragma unroll
      for (int i = 0; i < 4; i++) hb[c][i] = opaque(h0 + c * IMG_SZ - 1024 * i);
  }
  LAS char* const gb = opaque(S + LD_U + (li - lg + 16) * 16 - 64 * w);
  LAS char* const pxw = opaque(S + LD_PX + w * 2048 + lane * 16);
  LAS char* const pxw2 = opaque(pxw + 65536);
  const u32 voff = lane * 16;
  const char* const wp = ws + WS_W13 + (gj * 4 + w) * 61440;
  BARRIER();
  if (b == 0 && tid == 0) __builtin_amdgcn_raw_buffer_store_b32(tag, rws, WS_EPOCH, 0, SC1);
  STAMP_AT(5);
  u32x4 W[60];
  {
    u32x2 Hl[2][8], Hh[2][8];
    u32x4 G[2][8];
    auto rd = [&](auto C_, auto HH_, auto N_) {
      constexpr int c = C_, hh = HH_, n = N_, i = n / 3, k = n % 3;
      if constexpr (k == 0) G[hh][i] = *(LAS u32x4*)(gb + (2 * c + hh) * U_SZ + 256 * i);
      else {
        LAS u32* const p = (LAS u32*)(hb[c][i >> 1] + ((i & 1) ? 0 : 512) + 16 * hh + (k == 2 ? 8 : 0));
        const u32x2 v = {p[0], p[1]};
        if constexpr (k == 1) Hl[hh][i] = v; else Hh[hh][i] = v;
      }
    };
    sfor<0, 24>([&](auto N_) { rd(IC<0>{}, IC<0>{}, N_); });
    for (u32 spins = 0;; spins++) {
      if (__all(tf == tag) || spins > SPIN_CAP) break;
      tf = poll32(ws + WS_TFLAG + gj * 256 + li * 4);
    }
    asm volatile("" ::: "memory");
    STAMP_AT(19);
    f32x4 last7;
    auto channel = [&](auto C_) {
      constexpr int c = C_;
      f32x4 acc[8];
      auto putacc = [&](int cc, int al, const f32x4& v) {
        LAS char* const p = (cc == 2 ? pxw2 : pxw + cc * 32768) + (al >> 1) * 8192 + (al & 1) * 1024;
        *(LAS f32x4*)p = v;
      };
      sfor<0, 2>([&](auto HH_) {
        constexpr int hh = HH_;
        lgkwait<0>();
        SCHED();
        sfor<0, 36>([&](auto N_) {
          constexpr int n = N_;
          constexpr int al = n < 1 ? 0 : n < 3 ? 1 : n < 6 ? 2 : n < 10 ? 3 : n < 15 ? 4 : n < 21 ? 5 : n < 28 ? 6 : 7;
          constexpr int m1 = n - al * (al + 1) / 2;
          mf_hv<hh == 0 && m1 == 0>(acc[al], u32x4{Hl[hh][m1].x, Hl[hh][m1].y, Hh[hh][m1].x, Hh[hh][m1].y}, G[hh][al - m1]);
          if constexpr (n < 24) {
            if constexpr (hh == 0) rd(C_, IC<1>{}, N_);
            else if constexpr (c < 2) rd(IC<c + 1>{}, IC<0>{}, N_);
          }
          if constexpr (c < 2 && (n & 3) == 1) { constexpr int i = (2 * c + hh) * 9 + (n >> 2); LDW(W[i], voff, wp, i); }
          if constexpr (hh == 0 && c > 0 && n == 6) putacc(c - 1, 7, last7);
          if constexpr (hh == 1) {
            if constexpr (n == 10) putacc(c, 0, acc[0]);
            if constexpr (n == 14) putacc(c, 1, acc[1]);
            if constexpr (n == 18) putacc(c, 2, acc[2]);
            if constexpr (n == 22) putacc(c, 3, acc[3]);
            if constexpr (n == 26) putacc(c, 4, acc[4]);
            if constexpr (n == 30) putacc(c, 5, acc[5]);
            if constexpr (n == 35) putacc(c, 6, acc[6]);
          }
          SCHED();
        });
      });
      last7 = acc[7];
    };
    channel(IC<0>{});
    channel(IC<1>{});
    if (nch == 3) {
      channel(IC<2>{});
      asm volatile("s_nop 7" ::"v"(last7) : "memory");
      *(LAS f32x4*)(pxw2 + 3 * 8192 + 1024) = last7;
    } else {
      asm volatile("s_nop 7" ::"v"(last7) : "memory");
      *(LAS f32x4*)(pxw + 32768 + 3 * 8192 + 1024) = last7;
    }
  }
  BARRIER();
  STAMP_AT(6);
  {
    f32x4 pv[3][2][4];
    u32x2 xv[3][2];
    float inv[3];
#pragma unroll
    for (int c = 0; c < 3; c++) {
      inv[c] = *(LAS float*)(S + LD_MISC + c * 4);
#pragma unroll
      for (int t = 0; t < 2; t++) {
        LAS char* const p = S + LD_PX + c * 32768 + w * 8192 + t * 1024 + lane * 16;
#pragma unroll
        for (int v = 0; v < 4; v++) pv[c][t][v] = *(LAS f32x4*)(p + v * 2048);
        xv[c][t] = *(LAS u32x2*)(S + LD_XT + c * 4096 + 512 * (2 * w + t) + 32 * li + 8 * lg);
      }
    }
    SCHED();
    const int a = 16 * (2 * w + (lg & 1)) + li;
#pragma unroll
    for (int c = 0; c < 3; c++) {
      u32 o[2][2];
#pragma unroll
      for (int t = 0; t < 2; t++) {
        const f32x4 y = (pv[c][t][0] + pv[c][t][1]) + (pv[c][t][2] + pv[c][t][3]);
        o[t][0] = pkbf(fmaf(y[0], inv[c], bflo(xv[c][t].x)), fmaf(y[1], inv[c], bfhi(xv[c][t].x)));
        o[t][1] = pkbf(fmaf(y[2], inv[c], bflo(xv[c][t].y)), fmaf(y[3], inv[c], bfhi(xv[c][t].y)));
      }
      swap16(o[0][0], o[1][0]); swap16(o[0][1], o[1][1]);
      const int d = ch0 + c;
      const int off = (a >> 2) * 81920 + (((a & 3) ^ ((d >> 1) & 3)) * 32) + (lg >> 1) * 16;
      if (c < nch) __builtin_amdgcn_raw_buffer_store_b128(u32x4{o[0][0], o[0][1], o[1][0], o[1][1]}, rws, off + WS_HT + d * 128, 0, SC1);
    }
  }
  STAMP_AT(20);
  VMWAIT(0);
  BARRIER();
  if (tid < 8) __hip_atomic_fetch_add((GAS u32*)(ws + WS_CTRL + (tid * 8 + (slot & 7)) * 256), 1u, RLX_AGENT);
  STAMP_AT(7);
  sfor<NW_CONV, 60>([&](auto I) { constexpr int i = I; LDW(W[i], voff, wp, i); });
  f32x4 cs[3];
  const u32x4 on = opaque(u32x4{0x3f803f80u, 0x3f803f80u, 0x3f803f80u, 0x3f803f80u});
  const rsrc_t rht = __builtin_amdgcn_make_buffer_rsrc(ws + WS_HT + gr * 163840, 0, 163840, 0x00020000);
  LAS char* bp[3][4];
#pragma unroll
  for (int m = 0; m < 4; m++) {
    LAS char* const p = S + (lane >> 2) * 128 + (((m ^ w) ^ ((lane >> 3) & 3)) * 32) + (lane & 3) * 8;
#pragma unroll
    for (int k = 0; k < 3; k++) bp[k][m] = opaque(p + k * 65536);
  }
  asm volatile("s_nop 1");
  sfor<0, NW_CONV>([&](auto I) { constexpr int i = I; mf_bfa<(i < 3)>(cs[i % 3], W[i], on); });
  VMWAIT(0);
  sfor<NW_CONV, 60>([&](auto I) { constexpr int i = I; mf_bfa<false>(cs[i % 3], W[i], on); });
  STAMP_AT(21);
  for (u32 spins = 0;; spins++) {
    const u32 f = poll32(ws + WS_CTRL + (xcd * 8 + (lane & 7)) * 256);
    if (__all(f >= 32u) || spins > SPIN_CAP) break;
  }
  asm volatile("" ::: "memory");
  STAMP_AT(8);
  f32x4 acc1[2][4][3], sq[2], su[2];
  u32x4 V[60];
  {
    const u32 m0w = w * 1024;
    const char* const vp = ws + WS_W2 + (j2 * 4 + w) * 61440;
    sfor<0, 10>([&](auto I) { constexpr int s = I; set_m0n<s * 4096>(m0w); dma_m0(voff, rht); });
    u32x2 Bl[8], Bh[8];
    auto rdb = [&](auto F_, auto H_) {
      constexpr int f = F_, h = H_, s = f >> 2, m = f & 3, o = s * 4096 + h * 2048;
      const u32x2 v = tr64(bp[o >> 16][m] + (o & 65535));
      if constexpr (h == 0) Bl[f & 7] = v; else Bh[f & 7] = v;
    };
    vmwait<8>();
    __builtin_amdgcn_s_barrier();
    asm volatile("" ::: "memory");
    STAMP_AT(22);
    sfor<0, 6>([&](auto F_) { rdb(F_, IC<0>{}); rdb(F_, IC<1>{}); });
    sfor<0, 40>([&](auto S_) {
      constexpr int s = S_, hf = s / 20, ks = s % 20;
      lgkwait<(s < 39 ? 4 : 0)>();
      SCHED();
      sfor<0, 4>([&](auto M_) {
        constexpr int m = M_, f = 4 * s + m;
        if constexpr (m == 2) {
          if constexpr (s == 0) { vmwait<11 - 6>(); __builtin_amdgcn_s_barrier(); }
          if constexpr (s == 4) { vmwait<19 - 12>(); __builtin_amdgcn_s_barrier(); }
          if constexpr (s == 10) { vmwait<31 - 20>(); __builtin_amdgcn_s_barrier(); }
          if constexpr (s == 18) { vmwait<0>(); __builtin_amdgcn_s_barrier(); }
          asm volatile("" ::: "memory");
        }
        const u32x4 B = {Bl[f & 7].x, Bl[f & 7].y, Bh[f & 7].x, Bh[f & 7].y};
        mf_bfa<ks == 0>(acc1[hf][m][0], W[ks * 3], B);
        if constexpr (f + 6 < 160) rdb(IC<f + 6>{}, IC<0>{});
        if constexpr (s < 15 && (m & 1)) set_m0<(10 + 2 * s + (m >> 1)) * 4096>(m0w);
        SCHED();
        mf_bfa<ks == 0>(acc1[hf][m][1], W[ks * 3 + 1], B);
        if constexpr (f + 6 < 160) rdb(IC<f + 6>{}, IC<1>{});
        SCHED();
        mf_bfa<ks == 0>(acc1[hf][m][2], W[ks * 3 + 2], B);
        if constexpr (s < 15 && (m & 1)) dma_m0(voff, rht);
        if constexpr (s > 20 && m < 3) { constexpr int i = (s - 21) * 3 + m; LDW(V[i], voff, vp, i); }
        if constexpr (m == 0) { mf_bfv<ks == 0>(sq[hf], B, B); mf_bfv<ks == 0>(su[hf], on, B); }
        SCHED();
      });
    });
    sfor<57, 60>([&](auto I) { constexpr int i = I; LDW(V[i], voff, vp, i); });
  }
  STAMP_AT(9);
  u32x2 res[5];
  {
    const int hfw = w >> 1, mfw = 2 * sub + (w & 1);
#pragma unroll
    for (int t = 0; t < 5; t++) {
      const int row = 80 * j2 + (t < 4 ? 32 * (t >> 1) + 8 * lg + 4 * (t & 1) : 64 + 4 * lg) + ((lane >> 2) & 3);
      res[t] = tr64(S + hfw * 81920 + row * 128 + ((mfw ^ ((row >> 1) & 3)) * 32) + (lane & 3) * 8);
    }
  }
  LAS char* const stp = S + (j2 == 0 ? 40960 : 0);
  asm volatile("s_nop 7" : "+v"(sq[0]), "+v"(sq[1]), "+v"(su[0]), "+v"(su[1]));
  {
    f32x4 sv[2];
#pragma unroll
    for (int hf = 0; hf < 2; hf++) {
      const int e = li & 3;
      const float s2v = e == 0 ? sq[hf][0] : (e == 1 ? sq[hf][1] : (e == 2 ? sq[hf][2] : sq[hf][3]));
      const float mean = su[hf][0] * (1.f / D_);
      const float var = fmaxf(s2v * (1.f / D_) - mean * mean, 0.f);
      const float sd = rbf(__builtin_amdgcn_sqrtf(rbf(rbf(var) + 1e-5f)));
      sv[hf] = f32x4{-rbf(mean), -1.44269504088896341f * __builtin_amdgcn_rcpf(sd), sd * sd, 0.f};
    }
    if ((li >> 2) == lg) { *(LAS f32x4*)(stp + (w * 16 + li) * 16) = sv[0]; *(LAS f32x4*)(stp + 1024 + (w * 16 + li) * 16) = sv[1]; }
  }
  BARRIER();
  {
    f32x4 sv[2][4];
#pragma unroll
    for (int hf = 0; hf < 2; hf++)
#pragma unroll
      for (int m = 0; m < 4; m++) sv[hf][m] = *(LAS f32x4*)(stp + hf * 1024 + ((m ^ w) * 16 + li) * 16);
    SCHED();
#pragma unroll
    for (int hf = 0; hf < 2; hf++) {
#pragma unroll
      for (int t = 0; t < 3; t++) {
        u32 hv[4];
#pragma unroll
        for (int m = 0; m < 4; m++) {
          const f32x4 s4 = sv[hf][m];
          const f32x2 ga = __builtin_elementwise_fma(cs[t].xy, s4.xx, acc1[hf][m][t].xy);
          const f32x2 ua = __builtin_elementwise_fma(cs[t].zw, s4.xx, acc1[hf][m][t].zw);
          const f32x2 ar = ga * s4.yy;
          const f32x2 ex = {__builtin_amdgcn_exp2f(ar.x), __builtin_amdgcn_exp2f(ar.y)};
          const f32x2 dn = __builtin_elementwise_fma(ex, s4.zz, s4.zz);
          const f32x2 rc = {__builtin_amdgcn_rcpf(dn.x), __builtin_amdgcn_rcpf(dn.y)};
          const f32x2 p = (ga * ua) * rc;
          hv[m] = pkbf(p.x, p.y);
        }
        swap32(hv[0], hv[2]); swap32(hv[1], hv[3]);
        swap16(hv[0], hv[1]); swap16(hv[2], hv[3]);
        __builtin_amdgcn_raw_buffer_store_b128(u32x4{hv[0], hv[1], hv[2], hv[3]}, rws, ((lg ^ w) * 48 * 1024) + (w * 16 + li) * 16 +
                                               WS_HID + ((8 * gr + 4 * hf) * 48 + 3 * gj + t) * 1024, 0, SC1);
      }
    }
  }
  STAMP_AT(23);
  VMWAIT(0);
  if (lane == 0) __hip_atomic_fetch_add((GAS u32*)(ws + WS_CTRL + (64 + gr * 4 + (gj >> 2)) * 256), 1u, RLX_AGENT);
  STAMP_AT(10);
  int hoff[4];
#pragma unroll
  for (int m = 0; m < 4; m++) { const int q = m ^ w, mf = 8 * gr + 4 * (q >> 1) + 2 * sub + (q & 1); hoff[m] = WS_HID + (mf * 48 + 12 * w) * 1024; }
  LAS char* red[4];
#pragma unroll
  for (int m = 1; m < 4; m++) red[m] = opaque(S + 81920 + lane * 16 + ((m ^ w) * 3 + (m - 1)) * 5120);
  red[0] = opaque(S + 81920 + lane * 16 + w * 15360);
  for (u32 spins = 0;; spins++) {
    const u32 f = poll32(ws + WS_CTRL + (64 + gr * 4 + w) * 256);
    if (__all(f >= 16u) || spins > SPIN_CAP) break;
  }
  asm volatile("" ::: "memory");
  STAMP_AT(11);
  f32x4 acc2[4][5];
  {
    u32x4 Hf[12][4];
    constexpr int PRE = 9;
    sfor<0, PRE * 4>([&](auto I) { constexpr int ks = I / 4, m = I % 4; Hf[ks][m] = __builtin_amdgcn_raw_buffer_load_b128(rws, voff + (ks & 3) * 1024, hoff[m] + (ks >> 2) * 4096, SC1); SCHED(); });
    sfor<0, 9>([&](auto K_) {
      constexpr int ks = K_;
      sfor<0, 4>([&](auto M_) {
        constexpr int m = M_;
        sfor<0, 5>([&](auto T_) { constexpr int t = T_; mf_bfa<ks == 0>(acc2[m][t], V[ks * 5 + t], Hf[ks][m]); });
        if constexpr (ks + PRE < 12) Hf[ks + PRE][m] = __builtin_amdgcn_raw_buffer_load_b128(rws, voff + ((ks + PRE) & 3) * 1024, hoff[m] + ((ks + PRE) >> 2) * 4096, SC1);
        SCHED();
      });
    });
    sfor<0, 4>([&](auto Q_) {
      constexpr int q = Q_, m = (q + 1) & 3;
      sfor<0, 15>([&](auto N_) {
        constexpr int n = N_, ks = 9 + n / 5, t = n % 5;
        mf_bfa<false>(acc2[m][t], V[ks * 5 + t], Hf[ks][m]);
        if constexpr (q > 0 && n >= 4 && n < 14 && (n & 1) == 0) { constexpr int tt = (n - 4) / 2; *(LAS f32x4*)(red[q] + tt * 1024) = acc2[q][tt]; }
        SCHED();
      });
    });
  }
  STAMP_AT(12);
  {
    BARRIER();
    f32x4 pv[5][3];
#pragma unroll
    for (int t = 0; t < 5; t++)
#pragma unroll
      for (int v = 0; v < 3; v++) pv[t][v] = *(LAS f32x4*)(red[0] + t * 1024 + v * 5120);
    asm volatile("s_nop 1" : "+v"(acc2[0][0]), "+v"(acc2[0][1]), "+v"(acc2[0][2]), "+v"(acc2[0][3]), "+v"(acc2[0][4]));
    u32 o[5][2];
#pragma unroll
    for (int t = 0; t < 5; t++) {
      const f32x4 rv = {bflo(res[t].x), bfhi(res[t].x), bflo(res[t].y), bfhi(res[t].y)};
      const f32x4 v = ((acc2[0][t] + rv) + pv[t][0]) + (pv[t][1] + pv[t][2]);
      o[t][0] = pkbf(v[0], v[1]);
      o[t][1] = pkbf(v[2], v[3]);
    }
    const int l = 128 * gr + 64 * (w >> 1) + 16 * (2 * sub + (w & 1)) + li;
    u16* const op = out + l * D_ + 80 * j2;
    *(u32x4*)(op + 8 * lg) = u32x4{o[0][0], o[0][1], o[1][0], o[1][1]};
    *(u32x4*)(op + 32 + 8 * lg) = u32x4{o[2][0], o[2][1], o[3][0], o[3][1]};
    *(u32x2*)(op + 64 + 4 * lg) = u32x2{o[4][0], o[4][1]};
  }
  STAMP_AT(13);
  if (b == 100 && w == 0) { const u64 te = __builtin_amdgcn_s_memrealtime() + 10 * (ptb - pta); while (__builtin_amdgcn_s_memrealtime() < te) {} }
}

extern "C" void kernel_launch(void* const* d_in, const int* in_sizes, int n_in, void* d_out, int out_size, void* d_ws, size_t ws_size,
                              hipStream_t stream) {
  (void)hipFuncSetAttribute((const void*)fused_kernel, hipFuncAttributeMaxDynamicSharedMemorySize, LDS_BYTES);
  fused_kernel<<<256, 256, LDS_BYTES, stream>>>((const u16*)d_in[0], (const u16*)d_in[1], (const u16*)d_in[2], (const u16*)d_in[3], (const u16*)d_in[4],
      (const u16*)d_in[5], (const u16*)d_in[6], (const u16*)d_in[8], (const u16*)d_in[9], (const u16*)d_in[10], (u16*)d_out, (char*)d_ws);
}
```

```cpp
#define VARIANT 4
#include <hip/hip_runtime.h>
#include <stdint.h>

typedef unsigned short u16;
typedef unsigned u32;
typedef unsigned long long u64;
typedef u32 u32x2 __attribute__((ext_vector_type(2)));
typedef u32 u32x4 __attribute__((ext_vector_type(4)));
typedef float f32x2 __attribute__((ext_vector_type(2)));
typedef float f32x4 __attribute__((ext_vector_type(4)));
typedef float f32x16 __attribute__((ext_vector_type(16)));
typedef _Float16 f16x2 __attribute__((ext_vector_type(2)));
typedef __bf16 bf16x2 __attribute__((ext_vector_type(2)));
typedef short s16x4 __attribute__((ext_vector_type(4)));
#define LAS __attribute__((address_space(3)))
#define GAS __attribute__((address_space(1)))
#define DI __device__ __forceinline__
#define RLX_AGENT __ATOMIC_RELAXED, __HIP_MEMORY_SCOPE_AGENT
typedef __amdgpu_buffer_rsrc_t rsrc_t;

constexpr int L_ = 2048, D_ = 640, N_ = 192, F_ = 1536;
constexpr int SC1 = 16;
constexpr int CODE_PREFETCH_BYTES = 36864;
constexpr u32 SPIN_CAP = 1u << 18;

constexpr u32 WS_CTRL = 0;
constexpr u32 WS_EPOCH = 0x10000;
constexpr u32 WS_TFLAG = 0x11000;
constexpr u32 WS_XFLAG = 0x18000;
constexpr u32 WS_ST1 = 0x20000;
constexpr u32 WS_W13 = 0x100000;
constexpr u32 WS_W2 = 0x500000;
constexpr u32 WS_HT = 0x700000;
constexpr u32 WS_HID = 0xA00000;
constexpr u32 WS_DBG = 0x1000000;
constexpr u32 WS_XT = 0x1100000;
constexpr u32 WS_BYTES = 0x1400000;

constexpr int TROW = 2352;
constexpr int IMG_CP = 4160, IMG_SZ = 2 * IMG_CP;
constexpr int LD_PX = 3 * IMG_SZ;
constexpr int LD_U = 124800;
constexpr int U_SZ = 2368;
constexpr int LD_XT = LD_U + 6 * U_SZ;
constexpr int LD_KC = LD_XT + 3 * 4096;
constexpr int KC_SZ = 32 * 56;
constexpr int LD_MISC = LD_KC + 3 * KC_SZ;
constexpr int LD_TS = LD_U;
constexpr int LDS_BYTES = 163840;
static_assert(53 * TROW <= LD_U && LD_PX + 98304 <= LD_U, "");
static_assert(LD_TS + 10240 + 2 * 4224 + 5120 <= LD_KC, "");

template <int I> struct IC { static constexpr int value = I; constexpr operator int() const { return I; } };
template <int B, int E, class F> DI void sfor(F&& f) { if constexpr (B < E) { f(IC<B>{}); sfor<B + 1, E>(f); } }
template <int I> DI void ldw(u32x4& dst, u32 voff, const char* base) {
  asm volatile("global_load_dwordx4 %0, %1, %2 offset:%3 sc1" : "=a"(dst) : "v"(voff), "s"(base + (I >> 2) * 4096), "i"((I & 3) * 1024));
}
#define LDW(dst, voff, base, i) ldw<i>(dst, voff, base)
DI float asf(u32 u) { return __builtin_bit_cast(float, u); }
DI u32 asu(float f) { return __builtin_bit_cast(u32, f); }
DI float bflo(u32 u) { return asf(u << 16); }
DI float bfhi(u32 u) { return asf(u & 0xffff0000u); }
DI u32 pkbf(float a, float b) { f32x2 v = {a, b}; bf16x2 h = __builtin_convertvector(v, bf16x2); return __builtin_bit_cast(u32, h); }
DI u32 pkh(float a, float b) { f32x2 v = {a, b}; f16x2 h = __builtin_convertvector(v, f16x2); return __builtin_bit_cast(u32, h); }
DI u32 pkh2(f32x2 v) { f16x2 h = __builtin_convertvector(v, f16x2); return __builtin_bit_cast(u32, h); }
DI float rbf(float a) { return bflo(pkbf(a, a)); }
template <int CTRL> DI float dpp_add(float v) { return v + asf(__builtin_amdgcn_update_dpp(0, asu(v), CTRL, 0xf, 0xf, true)); }
template <int CTRL> DI u32 dpp_max(u32 v) { u32 o = __builtin_amdgcn_update_dpp(0, v, CTRL, 0xf, 0xf, true); return v > o ? v : o; }
DI f32x2 cmul(f32x2 a, f32x2 b, f32x2 bs) { f32x2 t = a.xx * b; return __builtin_elementwise_fma(a.yy, bs, t); }
DI u32x4 trfrag(LAS char* p0, LAS char* p1) {
  u32x2 lo = __builtin_bit_cast(u32x2, __builtin_amdgcn_ds_read_tr16_b64_v4i16((LAS s16x4*)p0));
  u32x2 hi = __builtin_bit_cast(u32x2, __builtin_amdgcn_ds_read_tr16_b64_v4i16((LAS s16x4*)p1));
  return u32x4{lo.x, lo.y, hi.x, hi.y};
}
DI u32x2 tr64(LAS char* p) { return __builtin_bit_cast(u32x2, __builtin_amdgcn_ds_read_tr16_b64_v4i16((LAS s16x4*)p)); }
DI void swap16(u32& a, u32& b) { auto r = __builtin_amdgcn_permlane16_swap(a, b, false, false); a = r[0]; b = r[1]; }
DI void swap32(u32& a, u32& b) { auto r = __builtin_amdgcn_permlane32_swap(a, b, false, false); a = r[0]; b = r[1]; }
#define VMWAIT(n) asm volatile("s_waitcnt vmcnt(%0)" ::"i"(n) : "memory")
#define BARRIER() do { asm volatile("s_waitcnt lgkmcnt(0)" ::: "memory"); __builtin_amdgcn_s_barrier(); asm volatile("" ::: "memory"); } while (0)
#define SCHED() __builtin_amdgcn_sched_barrier(0)

#define DEF_MFMA(name, op, T, CA) \
  template <bool Z> DI void name(T& acc, const u32x4& a, const u32x4& b) { \
    if constexpr (Z) asm volatile(op " %0, %1, %2, 0" : "=&v"(acc) : CA(a), "v"(b)); \
    else asm volatile(op " %0, %1, %2, %0" : "+v"(acc) : CA(a), "v"(b)); }
DEF_MFMA(mf_bfa, "v_mfma_f32_16x16x32_bf16", f32x4, "a")
DEF_MFMA(mf_bfv, "v_mfma_f32_16x16x32_bf16", f32x4, "v")
DEF_MFMA(mf_hv, "v_mfma_f32_16x16x32_f16", f32x4, "v")
DEF_MFMA(mf_h32, "v_mfma_f32_32x32x16_f16", f32x16, "v")
DI u32 poll32(const char* p) { return __hip_atomic_load((GAS u32*)p, RLX_AGENT); }
template <int N> DI void vmwait() { asm volatile("s_waitcnt vmcnt(%0)" ::"i"(N) : "memory"); }
template <int IMM> DI void set_m0(u32 base) { asm volatile("s_add_u32 m0, %0, %1" ::"s"(base), "i"(IMM) : "scc"); }
template <int IMM> DI void set_m0n(u32 base) { asm volatile("s_add_u32 m0, %0, %1\n\ts_nop 0" ::"s"(base), "i"(IMM) : "scc"); }
DI void dma_m0(u32 voff, rsrc_t r) { asm volatile("buffer_load_dwordx4 %0, %1, m0 offen sc1 lds" ::"v"(voff), "s"(r) : "memory"); }
template <int N> DI void lgkwait() { __builtin_amdgcn_s_waitcnt(0xC07F | (N << 8)); }
template <int O0, int O1, int O2, int O3> DI void put12(const u32 (&v)[4][3]) {
  asm volatile("ds_write_addtid_b32 %0 offset:%12\n\tds_write_addtid_b32 %1 offset:%12+256\n\tds_write_addtid_b32 %2 offset:%12+512\n\t"
               "ds_write_addtid_b32 %3 offset:%13\n\tds_write_addtid_b32 %4 offset:%13+256\n\tds_write_addtid_b32 %5 offset:%13+512\n\t"
               "ds_write_addtid_b32 %6 offset:%14\n\tds_write_addtid_b32 %7 offset:%14+256\n\tds_write_addtid_b32 %8 offset:%14+512\n\t"
               "ds_write_addtid_b32 %9 offset:%15\n\tds_write_addtid_b32 %10 offset:%15+256\n\tds_write_addtid_b32 %11 offset:%15+512"
               ::"v"(v[0][0]), "v"(v[0][1]), "v"(v[0][2]), "v"(v[1][0]), "v"(v[1][1]), "v"(v[1][2]), "v"(v[2][0]), "v"(v[2][1]), "v"(v[2][2]),
                 "v"(v[3][0]), "v"(v[3][1]), "v"(v[3][2]), "i"(O0), "i"(O1), "i"(O2), "i"(O3) : "memory");
}
template <int O0> DI void put3(const u32 (&v)[3]) {
  asm volatile("ds_write_addtid_b32 %0 offset:%3\n\tds_write_addtid_b32 %1 offset:%3+256\n\tds_write_addtid_b32 %2 offset:%3+512"
               ::"v"(v[0]), "v"(v[1]), "v"(v[2]), "i"(O0) : "memory");
}
template <class T> DI T opaque(T v) { asm volatile("" : "+v"(v)); return v; }

#define STAMP_AT(i) do {} while (0)
#define STAMP_W(i, t) do {} while (0)
#define PIN(v) do {} while (0)

DI f32x2 cexp(float ap, float bp) {
  const float e = __builtin_amdgcn_exp2f(ap * 1.44269504088896341f);
  const float C_HI = 0.15915494309189535f, C_LO = 6.4206382e-9f;
  const float x = bp * C_HI;
  const float n = __builtin_rintf(x);
  const float f = __builtin_fmaf(bp, C_LO, __builtin_fmaf(bp, C_HI, -n));
  return f32x2{e * __builtin_amdgcn_cosf(f), e * __builtin_amdgcn_sinf(f)};
}

DI u32 kc_index(int s) {
  const int p = (int)rbf((float)s);
  const u32 q = (u32)(p - 1);
  const u32 j = (q >> 3) & 31, sg = (q >> 8) & 7, c = q & 7;
  const u32 base = __builtin_amdgcn_perm(0x13121110u, 0x0E0C0800u, sg | 0x0c0c0c00u);
  const u32 sh = (0x33332210u >> (4 * sg)) & 3;
  u32 off = j * 56 + (base + (c >> sh)) * 2;
  off = s == 0 ? 40u : off;
  off = s < 0 ? 42u : off;
  return off;
}
DI float fexp(float x) { return __builtin_amdgcn_exp2f(x * 1.44269504088896341f); }

#define MAPL 1
constexpr int NW_CONV = 36;

__global__ __launch_bounds__(256, 1) void fused_kernel(const u16* __restrict__ x, const u16* __restrict__ lar, const u16* __restrict__ aim,
    const u16* __restrict__ cre, const u16* __restrict__ cim, const u16* __restrict__ ldt, const u16* __restrict__ dsk,
    const u16* __restrict__ w1, const u16* __restrict__ w3, const u16* __restrict__ w2, u16* __restrict__ out, char* __restrict__ ws) {
  extern __shared__ __attribute__((aligned(16))) char smem[];
  LAS char* const S = (LAS char*)smem;
  const u64 q_ts = __builtin_amdgcn_s_memrealtime();
  u64 pc;
  asm volatile("s_getpc_b64 %0" : "=s"(pc));
  asm volatile("" ::"s"(x), "s"(lar), "s"(aim), "s"(cre), "s"(cim), "s"(ldt), "s"(dsk), "s"(w1), "s"(w3), "s"(w2), "s"(out), "s"(ws));
  const int tid = threadIdx.x, lane = tid & 63, w = __builtin_amdgcn_readfirstlane(tid >> 6);
  const int b = blockIdx.x, xcd = b & 7, slot = b >> 3;
  const int li = lane & 15, lg = lane >> 4;
  const rsrc_t rws = __builtin_amdgcn_make_buffer_rsrc(ws, 0, WS_BYTES, 0x00020000);
  const rsrc_t rx = __builtin_amdgcn_make_buffer_rsrc((void*)x, 0, L_ * D_ * 2, 0x00020000);
  STAMP_AT(0);

  const int pr = xcd * 16 + (slot >> 1), odd = slot & 1;
  const int ch0 = 5 * pr + (odd ? 3 : 0), nch = odd ? 2 : 3;
  const int gr = (xcd >> MAPL) * (2 << MAPL) + (slot >> (4 - MAPL)), gj = (xcd & ((1 << MAPL) - 1)) * (16 >> MAPL) + (slot & ((16 >> MAPL) - 1));
  const int j2 = gj >> 1, sub = gj & 1;

  u32 tag, gidx[9];
  if (w < 3) {
    const int ch = ch0 + (w < nch ? w : 0);
    const rsrc_t r0 = __builtin_amdgcn_make_buffer_rsrc((void*)(lar + ch * N_), 0, N_ * 2, 0x00020000);
    const rsrc_t r1 = __builtin_amdgcn_make_buffer_rsrc((void*)(aim + ch * N_), 0, N_ * 2, 0x00020000);
    const rsrc_t r2 = __builtin_amdgcn_make_buffer_rsrc((void*)(cre + ch * N_), 0, N_ * 2, 0x00020000);
    const rsrc_t r3 = __builtin_amdgcn_make_buffer_rsrc((void*)(cim + ch * N_), 0, N_ * 2, 0x00020000);
    const rsrc_t r4 = __builtin_amdgcn_make_buffer_rsrc((void*)ldt, 0, D_ * 2, 0x00020000);
    const rsrc_t r5 = __builtin_amdgcn_make_buffer_rsrc((void*)dsk, 0, D_ * 2, 0x00020000);
    const u32 ldtv = __builtin_amdgcn_raw_buffer_load_b16(r4, ch * 2, 0, 0);
    const u32 la2 = __builtin_amdgcn_raw_buffer_load_b32(r0, lane * 4, 0, 0), la1 = __builtin_amdgcn_raw_buffer_load_b16(r0, 256 + lane * 2, 0, 0);
    const u32 ai2 = __builtin_amdgcn_raw_buffer_load_b32(r1, lane * 4, 0, 0), ai1 = __builtin_amdgcn_raw_buffer_load_b16(r1, 256 + lane * 2, 0, 0);
    const u32 cr2 = __builtin_amdgcn_raw_buffer_load_b32(r2, lane * 4, 0, 0), cr1 = __builtin_amdgcn_raw_buffer_load_b16(r2, 256 + lane * 2, 0, 0);
    const u32 ci2 = __builtin_amdgcn_raw_buffer_load_b32(r3, lane * 4, 0, 0), ci1 = __builtin_amdgcn_raw_buffer_load_b16(r3, 256 + lane * 2, 0, 0);
    const u32 dsv = __builtin_amdgcn_raw_buffer_load_b16(r5, ch * 2, 0, 0);
    tag = __builtin_amdgcn_raw_buffer_load_b32(rws, WS_EPOCH, 0, SC1);
    const rsrc_t rc = __builtin_amdgcn_make_buffer_rsrc((void*)(pc & ~255ull), 0, CODE_PREFETCH_BYTES, 0x00020000);
    const u32 cpf = __builtin_amdgcn_raw_buffer_load_b32(rc, (((slot & 1) * 3 + w) * 64 + lane) * 128, 0, 0);
    asm volatile("" ::: "memory");
    SCHED();
#pragma unroll
    for (int k = 0; k < 9; k++) gidx[k] = opaque(kc_index(2047 - 8 * tid - k));
    SCHED();
    STAMP_AT(26);
    PIN(ldtv); PIN(la2); PIN(la1); PIN(ai2); PIN(ai1);
    STAMP_AT(27);
    const float dt = rbf(fexp(bflo(ldtv)));
    f32x2 w1v[3], w8v[3], w256v[3], cc[3];
#pragma unroll
    for (int m = 0; m < 3; m++) {
      const float la = m == 0 ? bflo(la2) : (m == 1 ? bfhi(la2) : bflo(la1));
      const float ai = m == 0 ? bflo(ai2) : (m == 1 ? bfhi(ai2) : bflo(ai1));
      const float a = -rbf(fexp(la)) * dt, bb = ai * dt;
      w8v[m] = cexp(8.f * a, 8.f * bb);
      w1v[m] = cexp(a, -bb);
      w256v[m] = cexp(256.f * a, -256.f * bb);
    }
    PIN(w8v[0]); PIN(w8v[1]); PIN(w8v[2]); PIN(w1v[0]); PIN(w1v[1]); PIN(w1v[2]); PIN(w256v[0]); PIN(w256v[1]); PIN(w256v[2]);
    STAMP_AT(14);
    const u32 m0lo = w * 784;
    set_m0n<0>(m0lo);
    {
      f32x2 ws8[3], v[3];
      u32 pk[4][3];
#pragma unroll
      for (int m = 0; m < 3; m++) { ws8[m] = f32x2{-w8v[m].y, w8v[m].x}; v[m] = w8v[m]; pk[0][m] = 0x00003c00u; pk[1][m] = pkh2(v[m]); }
      sfor<0, 8>([&](auto G_) {
        constexpr int g = G_;
#pragma unroll
        for (int i = (g == 0 ? 2 : 0); i < 4; i++)
#pragma unroll
          for (int m = 0; m < 3; m++) { v[m] = cmul(v[m], w8v[m], ws8[m]); pk[i][m] = pkh2(v[m]); }
        if constexpr (g == 6) set_m0n<24 * TROW>(m0lo);
        constexpr int r0_ = g < 6 ? 4 * g : 4 * g - 24;
        put12<r0_ * TROW, (r0_ + 1) * TROW, (r0_ + 2) * TROW, (r0_ + 3) * TROW>(pk);
      });
    }
    STAMP_AT(28);
    u32 cm = cr2 & 0x7fffu;
    { u32 t;
      t = (cr2 >> 16) & 0x7fffu; cm = cm > t ? cm : t;
      t = ci2 & 0x7fffu; cm = cm > t ? cm : t;
      t = (ci2 >> 16) & 0x7fffu; cm = cm > t ? cm : t;
      t = cr1 & 0x7fffu; cm = cm > t ? cm : t;
      t = ci1 & 0x7fffu; cm = cm > t ? cm : t; }
    cm = dpp_max<0xB1>(cm); cm = dpp_max<0x4E>(cm); cm = dpp_max<0x141>(cm); cm = dpp_max<0x140>(cm);
    { u32 a0 = __builtin_amdgcn_readlane(cm, 0), a1 = __builtin_amdgcn_readlane(cm, 16), a2 = __builtin_amdgcn_readlane(cm, 32), a3 = __builtin_amdgcn_readlane(cm, 48);
      a0 = a0 > a1 ? a0 : a1; a2 = a2 > a3 ? a2 : a3; cm = a0 > a2 ? a0 : a2; }
    int ec = (int)(cm >> 7);
    ec = ec < 16 ? 16 : (ec > 254 ? 254 : ec);
    const float s1 = asf((u32)(127 + 13 + 127 - ec) << 23);
    const float s1i = asf((u32)(ec - 13) << 23);
    const float bound = 544.f * dt * bflo(cm) + fabsf(bflo(dsv));
    int eb = (int)(asu(bound) >> 23);
    eb = eb < 16 ? 16 : (eb > 254 ? 254 : eb);
    const float s2 = asf((u32)(127 + 14 + 127 - eb) << 23);
    const float s2i = asf((u32)(eb - 14) << 23);
    cc[0] = f32x2{bflo(cr2) * s1, -bflo(ci2) * s1}; cc[1] = f32x2{bfhi(cr2) * s1, -bfhi(ci2) * s1}; cc[2] = f32x2{bflo(cr1) * s1, -bflo(ci1) * s1};
    set_m0n<27 * TROW>(m0lo);
    {
      f32x2 ws1[3], ws256[3], v[3], k2[3], k4[3], k6[3];
      u32 pk[4][3];
#pragma unroll
      for (int m = 0; m < 3; m++) { ws1[m] = f32x2{-w1v[m].y, w1v[m].x}; ws256[m] = f32x2{-w256v[m].y, w256v[m].x}; v[m] = cc[m]; pk[0][m] = pkh2(v[m]); }
      put3<25 * TROW>(pk[0]);
#pragma unroll
      for (int i = 0; i < 4; i++)
#pragma unroll
        for (int m = 0; m < 3; m++) { v[m] = cmul(v[m], w1v[m], ws1[m]); pk[i][m] = pkh2(v[m]); if (i == 1) k2[m] = v[m]; if (i == 3) k4[m] = v[m]; }
      put12<5 * TROW, 6 * TROW, 7 * TROW, 8 * TROW>(pk);
#pragma unroll
      for (int i = 0; i < 4; i++)
#pragma unroll
        for (int m = 0; m < 3; m++) { v[m] = cmul(v[m], w1v[m], ws1[m]); pk[i][m] = pkh2(v[m]); if (i == 1) k6[m] = v[m]; }
      put12<9 * TROW, 10 * TROW, 11 * TROW, 12 * TROW>(pk);
#pragma unroll
      for (int m = 0; m < 3; m++) {
        k2[m] = cmul(k2[m], w256v[m], ws256[m]); pk[0][m] = pkh2(k2[m]);
        k4[m] = cmul(k4[m], w256v[m], ws256[m]); pk[1][m] = pkh2(k4[m]);
        k6[m] = cmul(k6[m], w256v[m], ws256[m]); pk[2][m] = pkh2(k6[m]);
        v[m] = cmul(v[m], w256v[m], ws256[m]); pk[3][m] = pkh2(v[m]);
      }
      put12<13 * TROW, 14 * TROW, 15 * TROW, 16 * TROW>(pk);
#pragma unroll
      for (int i = 0; i < 2; i++)
#pragma unroll
        for (int m = 0; m < 3; m++) {
          k4[m] = cmul(k4[m], w256v[m], ws256[m]); pk[2 * i][m] = pkh2(k4[m]);
          v[m] = cmul(v[m], w256v[m], ws256[m]); pk[2 * i + 1][m] = pkh2(v[m]);
        }
      put12<17 * TROW, 18 * TROW, 19 * TROW, 20 * TROW>(pk);
#pragma unroll
      for (int i = 0; i < 4; i++)
#pragma unroll
        for (int m = 0; m < 3; m++) { v[m] = cmul(v[m], w256v[m], ws256[m]); pk[i][m] = pkh2(v[m]); }
      put12<21 * TROW, 22 * TROW, 23 * TROW, 24 * TROW>(pk);
    }
    STAMP_AT(15);
    const int rr = lane & 31, hf = lane >> 5;
    LAS char* const pa = S + (32 + (rr > 20 ? 20 : rr)) * TROW + w * 784 + hf * 16;
    LAS char* const pb = S + rr * TROW + w * 784 + hf * 16;
    f32x16 kacc;
    u32x4 fa[24], fb[24];
    sfor<0, 28>([&](auto K_) {
      constexpr int ks = K_;
      if constexpr (ks < 24) { fa[ks] = *(LAS u32x4*)(pa + ks * 32); fb[ks] = *(LAS u32x4*)(pb + ks * 32); }
      if constexpr (ks >= 4) mf_h32<ks == 4>(kacc, fa[ks - 4], fb[ks - 4]);
      SCHED();
    });
    asm volatile("s_nop 7\n\ts_nop 7" : "+v"(kacc));
    const float fac = 2.f * dt * s1i * s2;
    LAS char* const kc = S + LD_KC + w * KC_SZ + rr * 56 + hf * 8;
    const float k8 = kacc[8] * fac + (hf ? bflo(dsv) * s2 : 0.f);
    const u32x2 q0 = {pkh(kacc[0] * fac, kacc[1] * fac), pkh(kacc[2] * fac, kacc[3] * fac)};
    const u32x2 q1 = {pkh(kacc[4] * fac, kacc[5] * fac), pkh(kacc[6] * fac, kacc[7] * fac)};
    const u32x2 q2 = {pkh(k8, hf ? 0.f : kacc[9] * fac), pkh(kacc[10] * fac, kacc[11] * fac)};
    *(LAS u32x2*)(kc) = q0; *(LAS u32x2*)(kc + 16) = q1; *(LAS u32x2*)(kc + 32) = q2;
    *(LAS float*)(S + LD_MISC + w * 4) = s2i;
    asm volatile("" ::"v"(cpf));
  } else {
    __builtin_amdgcn_raw_buffer_store_b32(0u, rws, WS_CTRL + b * 256 + lane * 4, 0, SC1);
    u32x4 xt[10], xr[10];
    int vo10[5];
#pragma unroll
    for (int i = 0; i < 5; i++) { const int id = lane + 64 * i, rho = id / 10, c = id - 10 * rho; vo10[i] = (rho * D_ + 8 * c) * 2; }
#pragma unroll
    for (int i = 0; i < 10; i++) xt[i] = __builtin_amdgcn_raw_buffer_load_b128(rx, vo10[i % 5], ((64 * slot + 32 * (i / 5)) * D_ + 80 * xcd) * 2, 0);
    const int row = 8 * b + (lane >> 3), part = lane & 7;
#pragma unroll
    for (int i = 0; i < 10; i++) xr[i] = __builtin_amdgcn_raw_buffer_load_b128(rx, row * (D_ * 2) + (part + 8 * i) * 16, 0, 0);
    tag = __builtin_amdgcn_raw_buffer_load_b32(rws, WS_EPOCH, 0, SC1);
    asm volatile("" ::: "memory");
    SCHED();
    const rsrc_t rw1 = __builtin_amdgcn_make_buffer_rsrc((void*)w1, 0, D_ * F_ * 2, 0x00020000);
    const rsrc_t rw3 = __builtin_amdgcn_make_buffer_rsrc((void*)w3, 0, D_ * F_ * 2, 0x00020000);
    const rsrc_t rw2 = __builtin_amdgcn_make_buffer_rsrc((void*)w2, 0, D_ * F_ * 2, 0x00020000);
    u32x4 wa[4][4], wb[2][5];
    const bool do13 = gr < 15;
    const int vo13 = ((lane >> 2) * F_ + 8 * (lane & 3)) * 2;
#pragma unroll
    for (int u = 0; u < 4; u++) {
      const int su = do13 ? 4 * gr + u : u, ks = su / 3, t = su - 3 * ks;
      const int so = (32 * ks * F_ + 96 * gj + 32 * t) * 2;
      wa[u][0] = __builtin_amdgcn_raw_buffer_load_b128(rw1, vo13, so, 0); wa[u][1] = __builtin_amdgcn_raw_buffer_load_b128(rw3, vo13, so, 0);
      wa[u][2] = __builtin_amdgcn_raw_buffer_load_b128(rw1, vo13, so + 32 * F_, 0); wa[u][3] = __builtin_amdgcn_raw_buffer_load_b128(rw3, vo13, so + 32 * F_, 0);
    }
#pragma unroll
    for (int i = 0; i < 5; i++) {
#pragma unroll
      for (int u = 0; u < 2; u++) {
        const int un = (u == 1 && b < 128) ? b + 256 : b, uj = un & 7, uk = un >> 3;
        wb[u][i] = __builtin_amdgcn_raw_buffer_load_b128(rw2, vo10[i], (32 * uk * D_ + 80 * uj) * 2, 0);
      }
    }
    asm volatile("" ::: "memory");
    SCHED();
#pragma unroll
    for (int k = 0; k < 9; k++) gidx[k] = opaque(kc_index(2047 - 8 * tid - k));
    SCHED();
    STAMP_W(29, 192);
    PIN(xt[9]);
    STAMP_W(30, 192);
    {
      LAS char* const XS = S + LD_TS;
#pragma unroll
      for (int i = 0; i < 10; i++) *(LAS u32x4*)(XS + (lane + 64 * i) * 16) = xt[i];
      LAS char* const p0 = XS + (8 * (4 * (lg & 1) + (lane & 3)) + ((lane >> 2) & 3)) * 160 + (lg >> 1) * 8;
      u32x4 fr[10];
#pragma unroll
      for (int k = 0; k < 10; k++) fr[k] = trfrag(p0 + k * 16, p0 + 640 + k * 16);
      const int xo = WS_XT + (20 * xcd + (lg >> 1)) * 16384 + slot * 512 + (lane & 31) * 16;
#pragma unroll
      for (int k = 0; k < 10; k++) __builtin_amdgcn_raw_buffer_store_b128(fr[k], rws, xo + k * 32768, 0, SC1);
      asm volatile("" ::: "memory");
      SCHED();
    }
    STAMP_W(24, 192);
    f32x2 xv[40], sm2 = {0.f, 0.f};
#pragma unroll
    for (int i = 0; i < 10; i++)
#pragma unroll
      for (int e = 0; e < 4; e++) { xv[4 * i + e] = f32x2{bflo(xr[i][e]), bfhi(xr[i][e])}; sm2 += xv[4 * i + e]; }
    float sm = sm2.x + sm2.y;
    sm = dpp_add<0xB1>(sm); sm = dpp_add<0x4E>(sm); sm = dpp_add<0x141>(sm);
    const float mean = sm * (1.f / D_);
    f32x2 q2 = {0.f, 0.f};
#pragma unroll
    for (int i = 0; i < 40; i++) { const f32x2 d = xv[i] - mean; q2 = __builtin_elementwise_fma(d, d, q2); }
    float q = q2.x + q2.y;
    q = dpp_add<0xB1>(q); q = dpp_add<0x4E>(q); q = dpp_add<0x141>(q);
    const float sd = __builtin_amdgcn_sqrtf(rbf(rbf(q * (1.f / D_)) + 1e-5f));
    if (part == 0) __builtin_amdgcn_raw_buffer_store_b64(u32x2{pkbf(mean, sd), tag + 1u}, rws, WS_ST1 + row * 8, 0, SC1);
    asm volatile("" ::: "memory");
    STAMP_W(31, 192);
    VMWAIT(1);
    if (lane == 0) __builtin_amdgcn_raw_buffer_store_b32(tag + 1u, rws, WS_XFLAG + (xcd * 32 + slot) * 128, 0, SC1);
    STAMP_W(16, 192);
    LAS char* TS = S + LD_TS + 10240;
    if (do13) {
#pragma unroll
      for (int u = 0; u < 4; u++) {
        const int su = 4 * gr + u, ks = su / 3, t = su - 3 * ks;
        TS = S + LD_TS + 10240 + (u & 1) * 4224;
        LAS char* const pw = TS + (lane & 3) * 1056 + (lane >> 2) * 32;
#pragma unroll
        for (int hh = 0; hh < 2; hh++) {
          const u32x4 g = wa[u][2 * hh], up = wa[u][2 * hh + 1];
          *(LAS u32x4*)(pw + hh * 512) = u32x4{g.x, up.x, g.y, up.y};
          *(LAS u32x4*)(pw + hh * 512 + 16) = u32x4{g.z, up.z, g.w, up.w};
        }
#pragma unroll
        for (int f = 0; f < 4; f++) {
          const u32x4 fr = trfrag(TS + f * 1056 + lane * 8, TS + f * 1056 + 512 + lane * 8);
          __builtin_amdgcn_raw_buffer_store_b128(fr, rws, lane * 16 + WS_W13 + (((gj * 4 + f) * 20 + ks) * 3 + t) * 1024, 0, SC1);
        }
      }
    }
#pragma unroll
    for (int u = 0; u < 2; u++) {
      if (u == 0 || b < 128) {
        const int un = u == 1 ? b + 256 : b, uj = un & 7, uk = un >> 3;
        TS = u == 0 ? S + LD_TS + 10240 + 2 * 4224 : S + LD_TS;
#pragma unroll
        for (int i = 0; i < 5; i++) *(LAS u32x4*)(TS + (lane + 64 * i) * 16) = wb[u][i];
        LAS char* const pr0 = TS + (8 * lg + ((lane >> 2) & 3)) * 160;
#pragma unroll
        for (int t = 0; t < 5; t++) {
          const int col = t < 4 ? 32 * (t >> 1) + 8 * (lane & 3) + 4 * (t & 1) : 64 + 4 * (lane & 3);
          const u32x4 fr = trfrag(pr0 + col * 2, pr0 + 640 + col * 2);
          __builtin_amdgcn_raw_buffer_store_b128(fr, rws, lane * 16 + WS_W2 + ((uj * 48 + uk) * 5 + t) * 1024, 0, SC1);
        }
      }
    }
    STAMP_W(17, 192);
    VMWAIT(0);
    if (lane == 0) __builtin_amdgcn_raw_buffer_store_b32(tag + 1u, rws, WS_TFLAG + gj * 256 + gr * 4, 0, SC1);
    STAMP_W(18, 192);
  }
  tag += 1u;
  STAMP_AT(1);
  u32x4 st[4];
#pragma unroll
  for (int i = 0; i < 4; i++) st[i] = __builtin_amdgcn_raw_buffer_load_b128(rws, WS_ST1 + tid * 64 + i * 16, 0, SC1);
  u32 xf = poll32(ws + WS_XFLAG + (xcd * 32 + w * 8 + (lane & 7)) * 128);
  BARRIER();
  STAMP_AT(2);
  for (u32 spins = 0;; spins++) {
    if (__all(xf == tag) || spins > SPIN_CAP) break;
    xf = poll32(ws + WS_XFLAG + (xcd * 32 + w * 8 + (lane & 7)) * 128);
  }
  asm volatile("" ::: "memory");
  u32x4 xc[3];
#pragma unroll
  for (int c = 0; c < 3; c++) { const int ch = ch0 + (c < nch ? c : 0); xc[c] = __builtin_amdgcn_raw_buffer_load_b128(rws, tid * 64, WS_XT + (ch >> 2) * 16384 + (ch & 3) * 16, SC1); }
  STAMP_AT(25);
  {
    u32 e[3][9];
#pragma unroll
    for (int c = 0; c < 3; c++)
#pragma unroll
      for (int k = 0; k < 9; k++) e[c][k] = *(LAS u16*)(S + LD_KC + c * KC_SZ + gidx[k]);
    SCHED();
    u32x4 p1[3], p0[3];
#pragma unroll
    for (int c = 0; c < 3; c++)
#pragma unroll
      for (int n = 0; n < 4; n++) { p1[c][n] = e[c][2 * n] | (e[c][2 * n + 1] << 16); p0[c][n] = e[c][2 * n + 1] | (e[c][2 * n + 2] << 16); }
    SCHED();
#pragma unroll
    for (int c = 0; c < 3; c++) { *(LAS u32x4*)(S + c * IMG_SZ + tid * 16) = p0[c]; *(LAS u32x4*)(S + c * IMG_SZ + IMG_CP + tid * 16) = p1[c]; }
    if (tid < 12) *(LAS u32x4*)(S + (tid >> 1) * IMG_CP + 4096 + (tid & 1) * 16) = u32x4{0, 0, 0, 0};
    if (tid >= 128 && tid < 224) *(LAS u32x4*)(S + LD_U + ((tid - 128) >> 4) * U_SZ + (tid & 15) * 16) = u32x4{0, 0, 0, 0};
  }
  STAMP_AT(3);
  for (u32 spins = 0;; spins++) {
    bool ok = true;
#pragma unroll
    for (int i = 0; i < 4; i++) ok = ok && st[i].y == tag && st[i].w == tag;
    if (__all(ok) || spins > SPIN_CAP) break;
    asm volatile("" ::: "memory");
#pragma unroll
    for (int i = 0; i < 4; i++) st[i] = __builtin_amdgcn_raw_buffer_load_b128(rws, WS_ST1 + tid * 64 + i * 16, 0, SC1);
  }
  u32 tf = poll32(ws + WS_TFLAG + gj * 256 + li * 4);
  STAMP_AT(4);
  {
    float ra[8], rb[8];
#pragma unroll
    for (int i = 0; i < 8; i++) { const u32 pl = (i & 1) ? st[i >> 1].z : st[i >> 1].x; ra[i] = __builtin_amdgcn_rcpf(bfhi(pl)); rb[i] = -bflo(pl) * ra[i]; }
    u32x4 v[3];
#pragma unroll
    for (int c = 0; c < 3; c++)
#pragma unroll
      for (int i = 0; i < 4; i++) {
        v[c][i] = pkh(fmaf(bflo(xc[c][i]), ra[2 * i], rb[2 * i]), fmaf(bfhi(xc[c][i]), ra[2 * i + 1], rb[2 * i + 1]));
      }
    SCHED();
#pragma unroll
    for (int c = 0; c < 3; c++) *(LAS u32x4*)(S + LD_U + (2 * c + (tid & 1)) * U_SZ + ((tid >> 1) + 16) * 16) = v[c];
#pragma unroll
    for (int c = 0; c < 3; c++) *(LAS u32x4*)(S + LD_XT + c * 4096 + tid * 16) = xc[c];
  }
  LAS char* hb[3][4];
  {
    LAS char* const h0 = S + (li & 1) * IMG_CP + 4092 - 32 * lg - 4 * (li >> 1) - 128 * w - 512;
#pragma unroll
    for (int c = 0; c < 3; c++)
#pragma unroll
      for (int i = 0; i < 4; i++) hb[c][i] = opaque(h0 + c * IMG_SZ - 1024 * i);
  }
  LAS char* const gb = opaque(S + LD_U + (li - lg + 16) * 16 - 64 * w);
  LAS char* const pxw = opaque(S + LD_PX + w * 2048 + lane * 16);
  LAS char* const pxw2 = opaque(pxw + 65536);
  const u32 voff = lane * 16;
  const char* const wp = ws + WS_W13 + (gj * 4 + w) * 61440;
  BARRIER();
  if (b == 0 && tid == 0) __builtin_amdgcn_raw_buffer_store_b32(tag, rws, WS_EPOCH, 0, SC1);
  STAMP_AT(5);
  u32x4 W[60];
  {
    u32x2 Hl[2][8], Hh[2][8];
    u32x4 G[2][8];
    auto rd = [&](auto C_, auto HH_, auto N_) {
      constexpr int c = C_, hh = HH_, n = N_, i = n / 3, k = n % 3;
      if constexpr (k == 0) G[hh][i] = *(LAS u32x4*)(gb + (2 * c + hh) * U_SZ + 256 * i);
      else {
        LAS u32* const p = (LAS u32*)(hb[c][i >> 1] + ((i & 1) ? 0 : 512) + 16 * hh + (k == 2 ? 8 : 0));
        const u32x2 v = {p[0], p[1]};
        if constexpr (k == 1) Hl[hh][i] = v; else Hh[hh][i] = v;
      }
    };
    sfor<0, 24>([&](auto N_) { rd(IC<0>{}, IC<0>{}, N_); });
    for (u32 spins = 0;; spins++) {
      if (__all(tf == tag) || spins > SPIN_CAP) break;
      tf = poll32(ws + WS_TFLAG + gj * 256 + li * 4);
    }
    asm volatile("" ::: "memory");
    STAMP_AT(19);
    f32x4 last7;
    auto channel = [&](auto C_) {
      constexpr int c = C_;
      f32x4 acc[8];
      auto putacc = [&](int cc, int al, const f32x4& v) {
        LAS char* const p = (cc == 2 ? pxw2 : pxw + cc * 32768) + (al >> 1) * 8192 + (al & 1) * 1024;
        *(LAS f32x4*)p = v;
      };
      sfor<0, 2>([&](auto HH_) {
        constexpr int hh = HH_;
        lgkwait<0>();
        SCHED();
        sfor<0, 36>([&](auto N_) {
          constexpr int n = N_;
          constexpr int al = n < 1 ? 0 : n < 3 ? 1 : n < 6 ? 2 : n < 10 ? 3 : n < 15 ? 4 : n < 21 ? 5 : n < 28 ? 6 : 7;
          constexpr int m1 = n - al * (al + 1) / 2;
          mf_hv<hh == 0 && m1 == 0>(acc[al], u32x4{Hl[hh][m1].x, Hl[hh][m1].y, Hh[hh][m1].x, Hh[hh][m1].y}, G[hh][al - m1]);
          if constexpr (n < 24) {
            if constexpr (hh == 0) rd(C_, IC<1>{}, N_);
            else if constexpr (c < 2) rd(IC<c + 1>{}, IC<0>{}, N_);
          }
          if constexpr (c < 2 && (n & 3) == 1) { constexpr int i = (2 * c + hh) * 9 + (n >> 2); LDW(W[i], voff, wp, i); }
          if constexpr (hh == 0 && c > 0 && n == 6) putacc(c - 1, 7, last7);
          if constexpr (hh == 1) {
            if constexpr (n == 10) putacc(c, 0, acc[0]);
            if constexpr (n == 14) putacc(c, 1, acc[1]);
            if constexpr (n == 18) putacc(c, 2, acc[2]);
            if constexpr (n == 22) putacc(c, 3, acc[3]);
            if constexpr (n == 26) putacc(c, 4, acc[4]);
            if constexpr (n == 30) putacc(c, 5, acc[5]);
            if constexpr (n == 35) putacc(c, 6, acc[6]);
          }
          SCHED();
        });
      });
      last7 = acc[7];
    };
    channel(IC<0>{});
    channel(IC<1>{});
    if (nch == 3) {
      channel(IC<2>{});
      asm volatile("s_nop 7" ::"v"(last7) : "memory");
      *(LAS f32x4*)(pxw2 + 3 * 8192 + 1024) = last7;
    } else {
      asm volatile("s_nop 7" ::"v"(last7) : "memory");
      *(LAS f32x4*)(pxw + 32768 + 3 * 8192 + 1024) = last7;
    }
  }
  BARRIER();
  STAMP_AT(6);
  {
    f32x4 pv[3][2][4];
    u32x2 xv[3][2];
    float inv[3];
#pragma unroll
    for (int c = 0; c < 3; c++) {
      inv[c] = *(LAS float*)(S + LD_MISC + c * 4);
#pragma unroll
      for (int t = 0; t < 2; t++) {
        LAS char* const p = S + LD_PX + c * 32768 + w * 8192 + t * 1024 + lane * 16;
#pragma unroll
        for (int v = 0; v < 4; v++) pv[c][t][v] = *(LAS f32x4*)(p + v * 2048);
        xv[c][t] = *(LAS u32x2*)(S + LD_XT + c * 4096 + 512 * (2 * w + t) + 32 * li + 8 * lg);
      }
    }
    SCHED();
    const int a = 16 * (2 * w + (lg & 1)) + li;
#pragma unroll
    for (int c = 0; c < 3; c++) {
      u32 o[2][2];
#pragma unroll
      for (int t = 0; t < 2; t++) {
        const f32x4 y = (pv[c][t][0] + pv[c][t][1]) + (pv[c][t][2] + pv[c][t][3]);
        o[t][0] = pkbf(fmaf(y[0], inv[c], bflo(xv[c][t].x)), fmaf(y[1], inv[c], bfhi(xv[c][t].x)));
        o[t][1] = pkbf(fmaf(y[2], inv[c], bflo(xv[c][t].y)), fmaf(y[3], inv[c], bfhi(xv[c][t].y)));
      }
      swap16(o[0][0], o[1][0]); swap16(o[0][1], o[1][1]);
      const int d = ch0 + c;
      const int off = (a >> 2) * 81920 + (((a & 3) ^ ((d >> 1) & 3)) * 32) + (lg >> 1) * 16;
      if (c < nch) __builtin_amdgcn_raw_buffer_store_b128(u32x4{o[0][0], o[0][1], o[1][0], o[1][1]}, rws, off + WS_HT + d * 128, 0, SC1);
    }
  }
  STAMP_AT(20);
  VMWAIT(0);
  BARRIER();
  if (tid < 8) __hip_atomic_fetch_add((GAS u32*)(ws + WS_CTRL + (tid * 8 + (slot & 7)) * 256), 1u, RLX_AGENT);
  STAMP_AT(7);
  sfor<NW_CONV, 60>([&](auto I) { constexpr int i = I; LDW(W[i], voff, wp, i); });
  f32x4 cs[3];
  const u32x4 on = opaque(u32x4{0x3f803f80u, 0x3f803f80u, 0x3f803f80u, 0x3f803f80u});
  const rsrc_t rht = __builtin_amdgcn_make_buffer_rsrc(ws + WS_HT + gr * 163840, 0, 163840, 0x00020000);
  LAS char* bp[3][4];
#pragma unroll
  for (int m = 0; m < 4; m++) {
    LAS char* const p = S + (lane >> 2) * 128 + (((m ^ w) ^ ((lane >> 3) & 3)) * 32) + (lane & 3) * 8;
#pragma unroll
    for (int k = 0; k < 3; k++) bp[k][m] = opaque(p + k * 65536);
  }
  asm volatile("s_nop 1");
  sfor<0, NW_CONV>([&](auto I) { constexpr int i = I; mf_bfa<(i < 3)>(cs[i % 3], W[i], on); });
  VMWAIT(0);
  sfor<NW_CONV, 60>([&](auto I) { constexpr int i = I; mf_bfa<false>(cs[i % 3], W[i], on); });
  STAMP_AT(21);
  for (u32 spins = 0;; spins++) {
    const u32 f = poll32(ws + WS_CTRL + (xcd * 8 + (lane & 7)) * 256);
    if (__all(f >= 32u) || spins > SPIN_CAP) break;
  }
  asm volatile("" ::: "memory");
  STAMP_AT(8);
  f32x4 acc1[2][4][3], sq[2], su[2];
  u32x4 V[60];
  {
    const u32 m0w = w * 1024;
    const char* const vp = ws + WS_W2 + (j2 * 4 + w) * 61440;
    sfor<0, 10>([&](auto I) { constexpr int s = I; set_m0n<s * 4096>(m0w); dma_m0(voff, rht); });
    u32x2 Bl[8], Bh[8];
    auto rdb = [&](auto F_, auto H_) {
      constexpr int f = F_, h = H_, s = f >> 2, m = f & 3, o = s * 4096 + h * 2048;
      const u32x2 v = tr64(bp[o >> 16][m] + (o & 65535));
      if constexpr (h == 0) Bl[f & 7] = v; else Bh[f & 7] = v;
    };
    vmwait<8>();
    __builtin_amdgcn_s_barrier();
    asm volatile("" ::: "memory");
    STAMP_AT(22);
    sfor<0, 6>([&](auto F_) { rdb(F_, IC<0>{}); rdb(F_, IC<1>{}); });
    sfor<0, 40>([&](auto S_) {
      constexpr int s = S_, hf = s / 20, ks = s % 20;
      lgkwait<(s < 39 ? 4 : 0)>();
      SCHED();
      sfor<0, 4>([&](auto M_) {
        constexpr int m = M_, f = 4 * s + m;
        if constexpr (m == 2) {
          if constexpr (s == 0) { vmwait<11 - 6>(); __builtin_amdgcn_s_barrier(); }
          if constexpr (s == 4) { vmwait<19 - 12>(); __builtin_amdgcn_s_barrier(); }
          if constexpr (s == 10) { vmwait<31 - 20>(); __builtin_amdgcn_s_barrier(); }
          if constexpr (s == 18) { vmwait<0>(); __builtin_amdgcn_s_barrier(); }
          asm volatile("" ::: "memory");
        }
        const u32x4 B = {Bl[f & 7].x, Bl[f & 7].y, Bh[f & 7].x, Bh[f & 7].y};
        mf_bfa<ks == 0>(acc1[hf][m][0], W[ks * 3], B);
        if constexpr (f + 6 < 160) rdb(IC<f + 6>{}, IC<0>{});
        if constexpr (s < 15 && (m & 1)) set_m0<(10 + 2 * s + (m >> 1)) * 4096>(m0w);
        SCHED();
        mf_bfa<ks == 0>(acc1[hf][m][1], W[ks * 3 + 1], B);
        if constexpr (f + 6 < 160) rdb(IC<f + 6>{}, IC<1>{});
        SCHED();
        mf_bfa<ks == 0>(acc1[hf][m][2], W[ks * 3 + 2], B);
        if constexpr (s < 15 && (m & 1)) dma_m0(voff, rht);
        if constexpr (s > 20 && m < 3) { constexpr int i = (s - 21) * 3 + m; LDW(V[i], voff, vp, i); }
        if constexpr (m == 0) { mf_bfv<ks == 0>(sq[hf], B, B); mf_bfv<ks == 0>(su[hf], on, B); }
        SCHED();
      });
    });
    sfor<57, 60>([&](auto I) { constexpr int i = I; LDW(V[i], voff, vp, i); });
  }
  STAMP_AT(9);
  u32x2 res[5];
  {
    const int hfw = w >> 1, mfw = 2 * sub + (w & 1);
#pragma unroll
    for (int t = 0; t < 5; t++) {
      const int row = 80 * j2 + (t < 4 ? 32 * (t >> 1) + 8 * lg + 4 * (t & 1) : 64 + 4 * lg) + ((lane >> 2) & 3);
      res[t] = tr64(S + hfw * 81920 + row * 128 + ((mfw ^ ((row >> 1) & 3)) * 32) + (lane & 3) * 8);
    }
  }
  LAS char* const stp = S + (j2 == 0 ? 40960 : 0);
  asm volatile("s_nop 7" : "+v"(sq[0]), "+v"(sq[1]), "+v"(su[0]), "+v"(su[1]));
  {
    f32x4 sv[2];
#pragma unroll
    for (int hf = 0; hf < 2; hf++) {
      const int e = li & 3;
      const float s2v = e == 0 ? sq[hf][0] : (e == 1 ? sq[hf][1] : (e == 2 ? sq[hf][2] : sq[hf][3]));
      const float mean = su[hf][0] * (1.f / D_);
      const float var = fmaxf(s2v * (1.f / D_) - mean * mean, 0.f);
      const float sd = rbf(__builtin_amdgcn_sqrtf(rbf(rbf(var) + 1e-5f)));
      sv[hf] = f32x4{-rbf(mean), -1.44269504088896341f * __builtin_amdgcn_rcpf(sd), sd * sd, 0.f};
    }
    if ((li >> 2) == lg) { *(LAS f32x4*)(stp + (w * 16 + li) * 16) = sv[0]; *(LAS f32x4*)(stp + 1024 + (w * 16 + li) * 16) = sv[1]; }
  }
  BARRIER();
  {
    f32x4 sv[2][4];
#pragma unroll
    for (int hf = 0; hf < 2; hf++)
#pragma unroll
      for (int m = 0; m < 4; m++) sv[hf][m] = *(LAS f32x4*)(stp + hf * 1024 + ((m ^ w) * 16 + li) * 16);
    SCHED();
#pragma unroll
    for (int hf = 0; hf < 2; hf++) {
#pragma unroll
      for (int t = 0; t < 3; t++) {
        u32 hv[4];
#pragma unroll
        for (int m = 0; m < 4; m++) {
          const f32x4 s4 = sv[hf][m];
          const f32x2 ga = __builtin_elementwise_fma(cs[t].xy, s4.xx, acc1[hf][m][t].xy);
          const f32x2 ua = __builtin_elementwise_fma(cs[t].zw, s4.xx, acc1[hf][m][t].zw);
          const f32x2 ar = ga * s4.yy;
          const f32x2 ex = {__builtin_amdgcn_exp2f(ar.x), __builtin_amdgcn_exp2f(ar.y)};
          const f32x2 dn = __builtin_elementwise_fma(ex, s4.zz, s4.zz);
          const f32x2 rc = {__builtin_amdgcn_rcpf(dn.x), __builtin_amdgcn_rcpf(dn.y)};
          const f32x2 p = (ga * ua) * rc;
          hv[m] = pkbf(p.x, p.y);
        }
        swap32(hv[0], hv[2]); swap32(hv[1], hv[3]);
        swap16(hv[0], hv[1]); swap16(hv[2], hv[3]);
        __builtin_amdgcn_raw_buffer_store_b128(u32x4{hv[0], hv[1], hv[2], hv[3]}, rws, ((lg ^ w) * 48 * 1024) + (w * 16 + li) * 16 +
                                               WS_HID + ((8 * gr + 4 * hf) * 48 + 3 * gj + t) * 1024, 0, SC1);
      }
    }
  }
  STAMP_AT(23);
  VMWAIT(0);
  if (lane == 0) __hip_atomic_fetch_add((GAS u32*)(ws + WS_CTRL + (64 + gr * 4 + (gj >> 2)) * 256), 1u, RLX_AGENT);
  STAMP_AT(10);
  int hoff[4];
#pragma unroll
  for (int m = 0; m < 4; m++) { const int q = m ^ w, mf = 8 * gr + 4 * (q >> 1) + 2 * sub + (q & 1); hoff[m] = WS_HID + (mf * 48 + 12 * w) * 1024; }
  LAS char* red[4];
#pragma unroll
  for (int m = 1; m < 4; m++) red[m] = opaque(S + 81920 + lane * 16 + ((m ^ w) * 3 + (m - 1)) * 5120);
  red[0] = opaque(S + 81920 + lane * 16 + w * 15360);
  for (u32 spins = 0;; spins++) {
    const u32 f = poll32(ws + WS_CTRL + (64 + gr * 4 + w) * 256);
    if (__all(f >= 16u) || spins > SPIN_CAP) break;
  }
  asm volatile("" ::: "memory");
  STAMP_AT(11);
  f32x4 acc2[4][5];
  {
    u32x4 Hf[12][4];
    constexpr int PRE = 9;
    sfor<0, PRE * 4>([&](auto I) { constexpr int ks = I / 4, m = I % 4; Hf[ks][m] = __builtin_amdgcn_raw_buffer_load_b128(rws, voff + (ks & 3) * 1024, hoff[m] + (ks >> 2) * 4096, SC1); SCHED(); });
    sfor<0, 9>([&](auto K_) {
      constexpr int ks = K_;
      sfor<0, 4>([&](auto M_) {
        constexpr int m = M_;
        sfor<0, 5>([&](auto T_) { constexpr int t = T_; mf_bfa<ks == 0>(acc2[m][t], V[ks * 5 + t], Hf[ks][m]); });
        if constexpr (ks + PRE < 12) Hf[ks + PRE][m] = __builtin_amdgcn_raw_buffer_load_b128(rws, voff + ((ks + PRE) & 3) * 1024, hoff[m] + ((ks + PRE) >> 2) * 4096, SC1);
        SCHED();
      });
    });
    sfor<0, 4>([&](auto Q_) {
      constexpr int q = Q_, m = (q + 1) & 3;
      sfor<0, 15>([&](auto N_) {
        constexpr int n = N_, ks = 9 + n / 5, t = n % 5;
        mf_bfa<false>(acc2[m][t], V[ks * 5 + t], Hf[ks][m]);
        if constexpr (q > 0 && n >= 4 && n < 14 && (n & 1) == 0) { constexpr int tt = (n - 4) / 2; *(LAS f32x4*)(red[q] + tt * 1024) = acc2[q][tt]; }
        SCHED();
      });
    });
  }
  STAMP_AT(12);
  {
    BARRIER();
    f32x4 pv[5][3];
#pragma unroll
    for (int t = 0; t < 5; t++)
#pragma unroll
      for (int v = 0; v < 3; v++) pv[t][v] = *(LAS f32x4*)(red[0] + t * 1024 + v * 5120);
    asm volatile("s_nop 1" : "+v"(acc2[0][0]), "+v"(acc2[0][1]), "+v"(acc2[0][2]), "+v"(acc2[0][3]), "+v"(acc2[0][4]));
    u32 o[5][2];
#pragma unroll
    for (int t = 0; t < 5; t++) {
      const f32x4 rv = {bflo(res[t].x), bfhi(res[t].x), bflo(res[t].y), bfhi(res[t].y)};
      const f32x4 v = ((acc2[0][t] + rv) + pv[t][0]) + (pv[t][1] + pv[t][2]);
      o[t][0] = pkbf(v[0], v[1]);
      o[t][1] = pkbf(v[2], v[3]);
    }
    const int l = 128 * gr + 64 * (w >> 1) + 16 * (2 * sub + (w & 1)) + li;
    u16* const op = out + l * D_ + 80 * j2;
    *(u32x4*)(op + 8 * lg) = u32x4{o[0][0], o[0][1], o[1][0], o[1][1]};
    *(u32x4*)(op + 32 + 8 * lg) = u32x4{o[2][0], o[2][1], o[3][0], o[3][1]};
    *(u32x2*)(op + 64 + 4 * lg) = u32x2{o[4][0], o[4][1]};
  }
  if (tid == 0) { atomicMax((unsigned long long*)(ws + WS_DBG + 8), (unsigned long long)__builtin_amdgcn_s_memrealtime()); atomicMin((unsigned long long*)(ws + WS_DBG + 16), (unsigned long long)q_ts); }
}

__global__ void k0(char* ws) { if (threadIdx.x == 0) { u64* p = (u64*)(ws + WS_DBG); p[0] = __builtin_amdgcn_s_memrealtime(); p[1] = 0; p[2] = ~0ull; } }
__global__ void k2(char* ws) {
  const u64 t = __builtin_amdgcn_s_memrealtime();
  const u64* p = (const u64*)(ws + WS_DBG);
  u64 X = 0;
  if (VARIANT == 1) X = p[2] - p[0];
  if (VARIANT == 2) X = t - p[1];
  if (VARIANT == 3) X = t - p[0];
  if (X > 100000) X = 100000;
  const u64 te = __builtin_amdgcn_s_memrealtime() + 10 * X;
  while (__builtin_amdgcn_s_memrealtime() < te) {}
}
extern "C" void kernel_launch(void* const* d_in, const int* in_sizes, int n_in, void* d_out, int out_size, void* d_ws, size_t ws_size,
                              hipStream_t stream) {
  (void)hipFuncSetAttribute((const void*)fused_kernel, hipFuncAttributeMaxDynamicSharedMemorySize, LDS_BYTES);
  k0<<<1, 64, 0, stream>>>((char*)d_ws);
  fused_kernel<<<256, 256, LDS_BYTES, stream>>>((const u16*)d_in[0], (const u16*)d_in[1], (const u16*)d_in[2], (const u16*)d_in[3], (const u16*)d_in[4],
      (const u16*)d_in[5], (const u16*)d_in[6], (const u16*)d_in[8], (const u16*)d_in[9], (const u16*)d_in[10], (u16*)d_out, (char*)d_ws);
  if (VARIANT != 4) k2<<<1, 64, 0, stream>>>((char*)d_ws);
}
```

```cpp
#include <hip/hip_runtime.h>
#include <stdint.h>

typedef unsigned short u16;
typedef unsigned u32;
typedef unsigned long long u64;
typedef u32 u32x2 __attribute__((ext_vector_type(2)));
typedef u32 u32x4 __attribute__((ext_vector_type(4)));
typedef float f32x2 __attribute__((ext_vector_type(2)));
typedef float f32x4 __attribute__((ext_vector_type(4)));
typedef float f32x16 __attribute__((ext_vector_type(16)));
typedef _Float16 f16x2 __attribute__((ext_vector_type(2)));
typedef __bf16 bf16x2 __attribute__((ext_vector_type(2)));
typedef short s16x4 __attribute__((ext_vector_type(4)));
#define LAS __attribute__((address_space(3)))
#define GAS __attribute__((address_space(1)))
#define DI __device__ __forceinline__
#define RLX_AGENT __ATOMIC_RELAXED, __HIP_MEMORY_SCOPE_AGENT
typedef __amdgpu_buffer_rsrc_t rsrc_t;

constexpr int L_ = 2048, D_ = 640, N_ = 192, F_ = 1536;
constexpr int SC1 = 16;
constexpr u32 SPIN_CAP = 1u << 18;

#define WS_BASE ((size_t)0 << 20)
constexpr u32 WS_CTRL = 0;
constexpr u32 WS_EPOCH = 0x10000;
constexpr u32 WS_TFLAG = 0x14000;
constexpr u32 WS_ST1 = 0x20000;
constexpr u32 WS_ST2 = 0x40000;
constexpr u32 WS_CS = 0x80000;
constexpr u32 WS_W13 = 0x100000;
constexpr u32 WS_W2 = 0x500000;
constexpr u32 WS_HT = 0x700000;
constexpr u32 WS_HID = 0xA00000;
constexpr u32 WS_DBG = 0x1000000;
constexpr u32 WS_XT = 0x1100000;
constexpr u32 WS_BYTES = 0x1400000;

constexpr int TROW = 784;
constexpr int TL_SZ = 32 * TROW, TH_SZ = 16512, LD_TH = 3 * TL_SZ;
constexpr int TH_M0 = 28672;
constexpr int IMG_CP = 4160, IMG_SZ = TL_SZ, LD_IMG = 0, LD_PX = LD_TH;
constexpr int LD_U = LD_TH + 3 * TH_SZ;
constexpr int U_SZ = 2368;
constexpr int LD_KC = LD_U + 6 * U_SZ;
constexpr int KC_SZ = 32 * 56;
constexpr int LD_MISC = LD_KC + 3 * KC_SZ;
constexpr int LD_TS = LD_MISC + 64;
constexpr int LDS_BYTES = 163840;
static_assert(21 * TROW <= TH_SZ && 4 * IMG_CP + 5120 <= IMG_SZ && 4 * 8320 <= 3 * TH_SZ, "");
static_assert(LD_TS + 10368 + 8 * 1056 <= LDS_BYTES, "");

template <int I> struct IC { static constexpr int value = I; constexpr operator int() const { return I; } };
template <int B, int E, class F> DI void sfor(F&& f) { if constexpr (B < E) { f(IC<B>{}); sfor<B + 1, E>(f); } }
template <int I> DI void ldw(u32x4& dst, u32 voff, const char* base) {
  asm volatile("global_load_dwordx4 %0, %1, %2 offset:%3 sc1" : "=a"(dst) : "v"(voff), "s"(base + (I >> 3) * 8192 + 4096), "i"((I & 7) * 1024 - 4096));
}
template <int I> DI void ldw_in(u32x4& dst, u32 voff, const char* base) {
  asm volatile("global_load_dwordx4 %0, %1, %2 offset:%3 sc1" : "+a"(dst) : "v"(voff), "s"(base + (I >> 3) * 8192 + 4096), "i"((I & 7) * 1024 - 4096));
}
#define NTV ""
#define NTH ""
template <int I> DI void ldv_a(u32x4& dst, u32 voff, const char* base) {
  asm volatile("global_load_dwordx4 %0, %1, %2 offset:%3 sc1" NTV : "=a"(dst) : "v"(voff), "s"(base + (I >> 3) * 8192 + 4096), "i"((I & 7) * 1024 - 4096));
}
template <int I> DI void ldw_v(u32x4& dst, u32 voff, const char* base) {
  asm volatile("global_load_dwordx4 %0, %1, %2 offset:%3 sc1" NTV : "=v"(dst) : "v"(voff), "s"(base + (I >> 3) * 8192 + 4096), "i"((I & 7) * 1024 - 4096));
}
DI void fresh_a(u32x4& v) { asm volatile("" : "=a"(v)); }
#define LDW(dst, voff, base, i) ldw<i>(dst, voff, base)
DI float asf(u32 u) { return __builtin_bit_cast(float, u); }
DI u32 asu(float f) { return __builtin_bit_cast(u32, f); }
DI float bflo(u32 u) { return asf(u << 16); }
DI float bfhi(u32 u) { return asf(u & 0xffff0000u); }
DI u32 pkbf(float a, float b) { f32x2 v = {a, b}; bf16x2 h = __builtin_convertvector(v, bf16x2); return __builtin_bit_cast(u32, h); }
DI u32 pkh(float a, float b) { f32x2 v = {a, b}; f16x2 h = __builtin_convertvector(v, f16x2); return __builtin_bit_cast(u32, h); }
DI u32 pkh2(f32x2 v) { f16x2 h = __builtin_convertvector(v, f16x2); return __builtin_bit_cast(u32, h); }
DI float rbf(float a) { return bflo(pkbf(a, a)); }
template <int CTRL> DI float dpp_add(float v) { return v + asf(__builtin_amdgcn_update_dpp(0, asu(v), CTRL, 0xf, 0xf, true)); }
template <int CTRL> DI u32 dpp_max(u32 v) { u32 o = __builtin_amdgcn_update_dpp(0, v, CTRL, 0xf, 0xf, true); return v > o ? v : o; }
template <int CTRL> DI u32 dpp(u32 v) { return __builtin_amdgcn_update_dpp(0, v, CTRL, 0xf, 0xf, true); }
DI f32x2 cmul(f32x2 a, f32x2 b, f32x2 bs) { f32x2 t = a.xx * b; return __builtin_elementwise_fma(a.yy, bs, t); }
DI u32x4 trfrag(LAS char* p0, LAS char* p1) {
  u32x2 lo = __builtin_bit_cast(u32x2, __builtin_amdgcn_ds_read_tr16_b64_v4i16((LAS s16x4*)p0));
  u32x2 hi = __builtin_bit_cast(u32x2, __builtin_amdgcn_ds_read_tr16_b64_v4i16((LAS s16x4*)p1));
  return u32x4{lo.x, lo.y, hi.x, hi.y};
}
DI u32x2 tr64(LAS char* p) { return __builtin_bit_cast(u32x2, __builtin_amdgcn_ds_read_tr16_b64_v4i16((LAS s16x4*)p)); }
DI void swap16(u32& a, u32& b) { auto r = __builtin_amdgcn_permlane16_swap(a, b, false, false); a = r[0]; b = r[1]; }
DI void swap32(u32& a, u32& b) { auto r = __builtin_amdgcn_permlane32_swap(a, b, false, false); a = r[0]; b = r[1]; }
#define VMWAIT(n) asm volatile("s_waitcnt vmcnt(%0)" ::"i"(n) : "memory")
#define BARRIER() do { asm volatile("s_waitcnt lgkmcnt(0)" ::: "memory"); __builtin_amdgcn_s_barrier(); asm volatile("" ::: "memory"); } while (0)
#define RB(r, i) asm volatile("; RB " #r " " #i)
#define RJ(r, i) asm volatile("; RJ " #r " " #i)
#define EB(r, i) asm volatile("; EB " #r " " #i)
#define EJ(r, i) asm volatile("; EJ " #r " " #i)
#define GSTAMP(i) do {} while (0)
#define PAGE_PAD(n, next) asm volatile("s_branch .Lgo" #n "\n\t.p2align 12\n.Lpad" #n ":\n\ts_cbranch_execz " #next "\n.Lgo" #n ":")
#define TOUCH_PAD() asm volatile("s_mov_b64 exec, 0\n\ts_branch .Lpad1\n.Lpad_ret:\n\ts_mov_b64 exec, -1")
#define PAGE_ALIGN(n) asm volatile("s_branch .Lal" #n "\n\t.p2align 12\n.Lal" #n ":")
#define SCHED() __builtin_amdgcn_sched_barrier(0)

#define DEF_MFMA(name, op, T, CA) \
  template <bool Z> DI void name(T& acc, const u32x4& a, const u32x4& b) { \
    if constexpr (Z) asm volatile(op " %0, %1, %2, 0" : "=&v"(acc) : CA(a), "v"(b)); \
    else asm volatile(op " %0, %1, %2, %0" : "+v"(acc) : CA(a), "v"(b)); }
DEF_MFMA(mf_bfa, "v_mfma_f32_16x16x32_bf16", f32x4, "a")
DEF_MFMA(mf_bfv, "v_mfma_f32_16x16x32_bf16", f32x4, "v")
#define DEF_MFMA2(name, CA) \
  template <bool Z> DI void name(f32x4& acc, const u32x4& a, const u32x4& b) { \
    if constexpr (Z) asm volatile("v_mfma_f32_16x16x32_bf16 %0, %2, %1, 0" : "=&v"(acc) : CA(a), "a"(b)); \
    else asm volatile("v_mfma_f32_16x16x32_bf16 %0, %2, %1, %0" : "+v"(acc) : CA(a), "a"(b)); }
DEF_MFMA2(mf_va, "v")
DEF_MFMA2(mf_aa, "a")
DEF_MFMA(mf_hv, "v_mfma_f32_16x16x32_f16", f32x4, "v")
DEF_MFMA(mf_h32, "v_mfma_f32_32x32x16_f16", f32x16, "v")
DI u32 poll32(const char* p) { return __hip_atomic_load((GAS u32*)p, RLX_AGENT); }
template <int N> DI void vmwait() { static_assert(N >= 0 && N < 64, ""); asm volatile("s_waitcnt vmcnt(%0)" ::"i"(N) : "memory"); }
template <int IMM> DI void set_m0(u32 base) { asm volatile("s_add_u32 m0, %0, %1" ::"s"(base), "i"(IMM) : "scc"); }
template <int IMM> DI void set_m0n(u32 base) { asm volatile("s_add_u32 m0, %0, %1\n\ts_nop 0" ::"s"(base), "i"(IMM) : "scc"); }
DI void dma_m0(u32 voff, rsrc_t r) { asm volatile("buffer_load_dwordx4 %0, %1, m0 offen sc1" NTH " lds" ::"v"(voff), "s"(r) : "memory"); }
template <int N> DI void lgkwait() { __builtin_amdgcn_s_waitcnt(0xC07F | (N << 8)); }
template <int O0, int O1, int O2, int O3> DI void put12(const u32 (&v)[4][3]) {
  asm volatile("ds_write_addtid_b32 %0 offset:%12\n\tds_write_addtid_b32 %1 offset:%12+256\n\tds_write_addtid_b32 %2 offset:%12+512\n\t"
               "ds_write_addtid_b32 %3 offset:%13\n\tds_write_addtid_b32 %4 offset:%13+256\n\tds_write_addtid_b32 %5 offset:%13+512\n\t"
               "ds_write_addtid_b32 %6 offset:%14\n\tds_write_addtid_b32 %7 offset:%14+256\n\tds_write_addtid_b32 %8 offset:%14+512\n\t"
               "ds_write_addtid_b32 %9 offset:%15\n\tds_write_addtid_b32 %10 offset:%15+256\n\tds_write_addtid_b32 %11 offset:%15+512"
               ::"v"(v[0][0]), "v"(v[0][1]), "v"(v[0][2]), "v"(v[1][0]), "v"(v[1][1]), "v"(v[1][2]), "v"(v[2][0]), "v"(v[2][1]), "v"(v[2][2]),
                 "v"(v[3][0]), "v"(v[3][1]), "v"(v[3][2]), "i"(O0), "i"(O1), "i"(O2), "i"(O3) : "memory");
}
template <int O> DI void put1(float v) { asm volatile("ds_write_addtid_b32 %0 offset:%1" ::"v"(v), "i"(O) : "memory"); }
template <int O0> DI void put3(const u32 (&v)[3]) {
  asm volatile("ds_write_addtid_b32 %0 offset:%3\n\tds_write_addtid_b32 %1 offset:%3+256\n\tds_write_addtid_b32 %2 offset:%3+512"
               ::"v"(v[0]), "v"(v[1]), "v"(v[2]), "i"(O0) : "memory");
}
#define NTX ""
#define NTW " nt"
#define NTW2 ""
#define NTP ""
#define S1_SLEEP0 0
#define S1_SLEEP 0
template <int OFF> DI void bld128w(u32x4& d, rsrc_t r, u32 vo, u32 so) { asm volatile("buffer_load_dwordx4 %0, %1, %2, %3 offen offset:%4" NTW : "+v"(d) : "v"(vo), "s"(r), "s"(so), "i"(OFF)); }
template <int OFF> DI void bld128(u32x4& d, rsrc_t r, u32 vo, u32 so) { asm volatile("buffer_load_dwordx4 %0, %1, %2, %3 offen offset:%4" : "+v"(d) : "v"(vo), "s"(r), "s"(so), "i"(OFF)); }
template <int OFF> DI void bld32(u32& d, rsrc_t r, u32 vo, u32 so) { asm volatile("buffer_load_dword %0, %1, %2, %3 offen offset:%4" : "+v"(d) : "v"(vo), "s"(r), "s"(so), "i"(OFF)); }
template <int OFF> DI void bld32p(u32& d, rsrc_t r, u32 vo, u32 so) { asm volatile("buffer_load_dword %0, %1, %2, %3 offen offset:%4" NTP : "+v"(d) : "v"(vo), "s"(r), "s"(so), "i"(OFF)); }
template <int OFF> DI void bld16(u32& d, rsrc_t r, u32 vo, u32 so) { asm volatile("buffer_load_ushort %0, %1, %2, %3 offen offset:%4" NTP : "+v"(d) : "v"(vo), "s"(r), "s"(so), "i"(OFF)); }
template <int OFF> DI void bld128_sc1(u32x4& d, rsrc_t r, u32 vo, u32 so) { asm volatile("buffer_load_dwordx4 %0, %1, %2, %3 offen offset:%4 sc1" : "+v"(d) : "v"(vo), "s"(r), "s"(so), "i"(OFF) : "memory"); }
DI void gld128w(u32x4& d, const void* p, u32 vo) { asm volatile("global_load_dwordx4 %0, %1, %2" NTW : "+v"(d) : "v"(vo), "s"(p)); }
DI void gld32(u32& d, const void* p, u32 vo) { asm volatile("global_load_dword %0, %1, %2" : "+v"(d) : "v"(vo), "s"(p)); }
DI void gld16(u32& d, const void* p, u32 vo) { asm volatile("global_load_ushort %0, %1, %2" : "+v"(d) : "v"(vo), "s"(p)); }
DI void gld32_sc1(u32& d, const void* p, u32 vo) { asm volatile("global_load_dword %0, %1, %2 sc1" : "+v"(d) : "v"(vo), "s"(p)); }
template <int P> DI void gtab_ld(u32x4 (&g)[5], u32 vo, u32 skip);
DI void bld32_sc1(u32& d, rsrc_t r, u32 vo) { asm volatile("buffer_load_dword %0, %1, %2, 0 offen sc1" : "+v"(d) : "v"(vo), "s"(r)); }
DI float s_add(float a, float b) { float r; asm volatile("v_add_f32 %0, %1, %2" : "=v"(r) : "v"(a), "v"(b)); return r; }
DI float s_mul(float a, float b) { float r; asm volatile("v_mul_f32 %0, %1, %2" : "=v"(r) : "v"(a), "v"(b)); return r; }
DI float s_fma(float a, float b, float c) { float r; asm volatile("v_fma_f32 %0, %1, %2, %3" : "=v"(r) : "v"(a), "v"(b), "v"(c)); return r; }
DI float s_exp2(float a) { float r; asm volatile("v_exp_f32 %0, %1" : "=v"(r) : "v"(a)); return r; }
DI float s_rcp(float a) { float r; asm volatile("v_rcp_f32 %0, %1" : "=v"(r) : "v"(a)); return r; }
DI void s_swap32(u32& a, u32& b) { asm volatile("v_permlane32_swap_b32 %0, %1" : "+v"(a), "+v"(b)); }
DI void s_swap16(u32& a, u32& b) { asm volatile("v_permlane16_swap_b32 %0, %1" : "+v"(a), "+v"(b)); }
DI u32 s_pkbf(float a, float b) { u32 r; asm volatile("v_cvt_pk_bf16_f32 %0, %1, %2" : "=v"(r) : "v"(a), "v"(b)); return r; }
template <class T> DI void tie(T& v) { asm volatile("" : "+v"(v)); }
template <class T> DI void fresh(T& v) { asm volatile("" : "=v"(v)); }
template <class T> DI T opaque(T v) { asm volatile("" : "+v"(v)); return v; }

#define PIN(v) do {} while (0)
#define STAMP_AT(i) do {} while (0)

DI f32x2 cexp(float ap, float bp) {
  const float e = __builtin_amdgcn_exp2f(ap * 1.44269504088896341f);
  const float C_HI = 0.15915494309189535f, C_LO = 6.4206382e-9f;
  const float x = bp * C_HI;
  const float n = __builtin_rintf(x);
  const float f = __builtin_fmaf(bp, C_LO, __builtin_fmaf(bp, C_HI, -n));
  return f32x2{e * __builtin_amdgcn_cosf(f), e * __builtin_amdgcn_sinf(f)};
}

constexpr u32 kc_index(int s) {
  if (s < 0) return LD_KC + 42;
  if (s == 0) return LD_KC + 40;
  int sh = 0;
  while ((s >> sh) >= 256) sh++;
  int p = s;
  if (sh) { const int lo = s & ((1 << sh) - 1), half = 1 << (sh - 1); p = s >> sh; if (lo > half || (lo == half && (p & 1))) p++; p <<= sh; }
  const int q = p - 1, j = (q >> 3) & 31, sg = q >> 8, c = q & 7;
  const int i = sg == 0 ? c : (sg == 1 ? 8 + (c >> 1) : (sg < 4 ? 8 + 2 * sg + (c >> 2) : 12 + sg));
  return LD_KC + j * 56 + i * 2;
}
constexpr int GB[20] = {1, 1, 1, 1, 2, 2, 2, 2, 2, 3, 3, 3, 3, 3, 3, 3, 3, 3, 3, 3}, GK[20] = {0, 3, 4, 10, 0, 2, 5, 9, 10, 0, 1, 2, 3, 4, 5, 6, 7, 8, 9, 10};
constexpr int gsel(int bl, int k) {
  return bl == 0 ? (k < 3 ? 0 : k == 3 ? 1 : 2) : bl == 1 ? (k < 3 ? 3 : k == 3 ? 4 : k < 10 ? 5 : 6) : bl == 2 ? (k < 2 ? 7 : k < 5 ? 8 : k < 9 ? 9 : k + 1) : 12 + k;
}
struct GTab { u32 v[3 * 20 * 64]; };
constexpr GTab make_gtab() {
  GTab t{};
  for (int c = 0; c < 3; c++) for (int n = 0; n < 64; n++) for (int e = 0; e < 20; e++) t.v[c * 1280 + (e >> 2) * 256 + n * 4 + (e & 3)] = c * KC_SZ + kc_index(2047 - 8 * (n + 64 * GB[e]) - GK[e]);
  return t;
}
constexpr bool check_gtab() {
  for (int n = 0; n < 64; n++) for (int bl = 0; bl < 4; bl++) for (int k = 0; k < 11; k++) {
    const int v = gsel(bl, k), e = v < 3 ? v : v - 3;
    if (kc_index(2047 - 8 * (n + 64 * bl) - k) != kc_index(2047 - 8 * (n + 64 * GB[e]) - GK[e]) + (v < 3 ? 4 : 0)) return false;
  }
  return true;
}
static_assert(check_gtab(), "");
__device__ const GTab g_gtab = make_gtab();
constexpr int ochunk(int g) { return 2 * (g & 1) + (g >> 1); }
struct RTab { u32 v[8 * 2 * 6 * 64]; };
constexpr RTab make_rtab() {
  RTab t{};
  for (int j2 = 0; j2 < 8; j2++) for (int wp = 0; wp < 2; wp++) for (int hf = 0; hf < 2; hf++) for (int k = 0; k < 3; k++) for (int l = 0; l < 64; l++) {
    const int g = l >> 4, q = (l >> 2) & 3;
    const int d = 80 * j2 + (k < 2 ? 32 * wp + 8 * ochunk(g) + 4 * k + q : 64 + 4 * g + q);
    const int p5 = d / 5, r5 = d % 5, c = r5 < 3 ? r5 : r5 - 3, kap = c < 2 ? 256 * c + 2 * p5 + (r5 > 2) : 512 + p5;
    t.v[((j2 * 2 + wp) * 6 + hf * 3 + k) * 64 + l] = ((kap >> 5) + (c ? 8 : 0) + hf * (c ? 12 : 8)) * 4096 + (kap & 31) * 128 + ((kap >> 1) & 3) * 32 + (l & 3) * 8;
  }
  return t;
}
__device__ const RTab g_rtab = make_rtab();
template <int P> DI void gtab_ld(u32x4 (&g)[5], u32 vo, u32 skip) {
  asm volatile("s_cmp_lg_u32 %7, 0\n\ts_cbranch_scc1 1f\n\t; EB %8 21\n\tglobal_load_dwordx4 %0, %5, %6 offset:-2048\n\tglobal_load_dwordx4 %1, %5, %6 offset:-1024\n\t"
               "global_load_dwordx4 %2, %5, %6\n\tglobal_load_dwordx4 %3, %5, %6 offset:1024\n\tglobal_load_dwordx4 %4, %5, %6 offset:2048\n1:\n\t; EJ %8 21"
               : "+v"(g[0]), "+v"(g[1]), "+v"(g[2]), "+v"(g[3]), "+v"(g[4]) : "v"(vo), "s"(&g_gtab), "s"(skip), "i"(P) : "scc");
}
DI float dot2(u32 a, u32 b, float c) { return __builtin_amdgcn_fdot2_f32_bf16(__builtin_bit_cast(bf16x2, a), __builtin_bit_cast(bf16x2, b), c, false); }
template <int OFF> DI void add_one(rsrc_t r, u32 vo) {
  asm volatile("s_mov_b64 exec, 3\n\tbuffer_atomic_add %0, %1, %2, %3 offen\n\ts_mov_b64 exec, -1" ::"v"(1u), "v"(vo), "s"(r), "s"(OFF) : "memory");
}
DI void add_lane0(rsrc_t r, u32 vo, u32 so) {
  asm volatile("s_mov_b64 exec, 1\n\tbuffer_atomic_add %0, %1, %2, %3 offen\n\ts_mov_b64 exec, -1" ::"v"(1u), "v"(vo), "s"(r), "s"(so) : "memory");
}
template <int OFF> DI void hld(u32x4& d, rsrc_t r, u32 vo, u32 so) { asm volatile("buffer_load_dwordx4 %0, %1, %2, %3 offen offset:%4 sc1" : "=v"(d) : "v"(vo), "s"(r), "s"(so), "i"(OFF)); }
template <int OFF> DI void hld_a(u32x4& d, rsrc_t r, u32 vo, u32 so) { asm volatile("buffer_load_dwordx4 %0, %1, %2, %3 offen offset:%4 sc1" : "=a"(d) : "v"(vo), "s"(r), "s"(so), "i"(OFF)); }
template <int OFF> DI void poll_ld(u32& d, rsrc_t r, u32 vo) { asm volatile("buffer_load_dword %0, %1, %2, %3 offen sc1" : "=v"(d) : "v"(vo), "s"(r), "s"(OFF) : "memory"); }
template <int OFF> DI void poll_in(u32& d, rsrc_t r, u32 vo) { asm volatile("buffer_load_dword %0, %1, %2, %3 offen sc1" : "+v"(d) : "v"(vo), "s"(r), "s"(OFF) : "memory"); }
template <int OFF, int WAITN, bool EQ> DI void poll_spin(u32& f, rsrc_t r, u32 vo, u32 want) {
  static_assert(WAITN >= 0 && WAITN < 64, "");
  u32 n;
#define POLL_SPIN(CMP) \
  asm volatile("s_waitcnt vmcnt(%6)\n\t" CMP " vcc, %5, %0\n\ts_cbranch_vccz 2f\n\t" \
               "s_mov_b32 %1, 0x40000\n3:\n\ts_sleep 2\n\tbuffer_load_dword %0, %2, %3, %4 offen sc1\n\ts_waitcnt vmcnt(0)\n\t" \
               CMP " vcc, %5, %0\n\ts_cbranch_vccz 2f\n\ts_sub_u32 %1, %1, 1\n\ts_cbranch_scc0 3b\n2:" \
               : "+v"(f), "=&s"(n) : "v"(vo), "s"(r), "s"(OFF), "v"(want), "i"(WAITN) : "memory", "vcc", "scc")
  if constexpr (EQ) POLL_SPIN("v_cmp_ne_u32"); else POLL_SPIN("v_cmp_gt_u32");
#undef POLL_SPIN
}
template <int OFF> DI void tab_ld(u32& d, rsrc_t r, u32 vo, u32 so) { asm volatile("buffer_load_dword %0, %1, %2, %3 offen offset:%4" : "=v"(d) : "v"(vo), "s"(r), "s"(so), "i"(OFF)); }
DI void xor_in(u32& d, u32 sv) { asm volatile("v_xor_b32 %0, %1, %0" : "+v"(d) : "s"(sv)); }
DI void dma_plain(u32 voff, rsrc_t r, u32 so) { asm volatile("buffer_load_dwordx4 %0, %1, %2 offen" NTX " lds" ::"v"(voff), "s"(r), "s"(so) : "memory"); }
template <int M0, int OFF> DI void dma_x(u32 voff, rsrc_t r, u32 so) {
  asm volatile("s_mov_b32 m0, %4\n\ts_nop 0\n\tbuffer_load_dwordx4 %0, %1, %2 offen offset:%3 lds" ::"v"(voff), "s"(r), "s"(so), "i"(OFF), "i"(M0) : "memory");
}
DI void dma_nt(u32 voff, rsrc_t r, u32 so) { asm volatile("buffer_load_dwordx4 %0, %1, %2 offen" NTW2 " lds" ::"v"(voff), "s"(r), "s"(so) : "memory"); }
DI void dma_so(u32 voff, rsrc_t r, u32 so) { asm volatile("buffer_load_dwordx4 %0, %1, %2 offen sc1 lds" ::"v"(voff), "s"(r), "s"(so) : "memory"); }
template <int WAITN> DI void rec_check(u32x4 (&d)[4], u32 tag, rsrc_t r, u32 vo, u32 so) {
  static_assert(WAITN >= 0 && WAITN < 64, "");
  asm volatile("s_waitcnt vmcnt(%4)" : "+v"(d[0]), "+v"(d[1]), "+v"(d[2]), "+v"(d[3]) : "i"(WAITN) : "memory");
  u32 bad = (((d[0].y ^ tag) | (d[0].w ^ tag)) | ((d[1].y ^ tag) | (d[1].w ^ tag))) | (((d[2].y ^ tag) | (d[2].w ^ tag)) | ((d[3].y ^ tag) | (d[3].w ^ tag)));
  asm volatile("" : "+v"(bad));
  if (__builtin_amdgcn_ballot_w64(bad != 0) != 0) {
    for (u32 spins = 0; spins < SPIN_CAP; spins++) {
      asm volatile("s_sleep 1\n\tbuffer_load_dwordx4 %0, %4, %5, %6 offen sc1\n\tbuffer_load_dwordx4 %1, %4, %5, %6 offen offset:256 sc1\n\t"
                   "buffer_load_dwordx4 %2, %4, %5, %6 offen offset:512 sc1\n\tbuffer_load_dwordx4 %3, %4, %5, %6 offen offset:768 sc1\n\ts_waitcnt vmcnt(0)"
                   : "+v"(d[0]), "+v"(d[1]), "+v"(d[2]), "+v"(d[3]) : "v"(vo), "s"(r), "s"(so) : "memory");
      bad = (((d[0].y ^ tag) | (d[0].w ^ tag)) | ((d[1].y ^ tag) | (d[1].w ^ tag))) | (((d[2].y ^ tag) | (d[2].w ^ tag)) | ((d[3].y ^ tag) | (d[3].w ^ tag)));
      if (__builtin_amdgcn_ballot_w64(bad != 0) == 0) break;
    }
  }
}
template <int K> DI void rec_step(const f32x4 (&p)[2], float& a, float& b, float& c, u32& c1, u32& ms) {
  if constexpr (K == 0) asm volatile("v_add_f32 %0, %2, %3\n\tv_add_f32 %1, %4, %5" : "=&v"(a), "=&v"(b) : "v"(p[0].x), "v"(p[0].z), "v"(p[0].y), "v"(p[0].w));
  if constexpr (K == 1) asm volatile("v_add_f32 %0, %0, %2\n\tv_add_f32 %1, %1, %3" : "+v"(a), "+v"(b) : "v"(p[1].x), "v"(p[1].y));
  if constexpr (K == 2) asm volatile("v_add_f32 %0, %0, %2\n\tv_add_f32 %1, %1, %3" : "+v"(a), "+v"(b) : "v"(p[1].z), "v"(p[1].w));
  if constexpr (K == 3) asm volatile("v_mul_f32 %0, 0x3acccccd, %0\n\tv_mul_f32 %1, 0xbacccccd, %1" : "+v"(a), "+v"(b));
  if constexpr (K == 4) asm volatile("v_fma_f32 %0, -%1, %1, %0\n\tv_max_f32 %0, 0, %0" : "+v"(a) : "v"(b));
  if constexpr (K == 5) asm volatile("v_cvt_pk_bf16_f32 %0, %0, %0\n\tv_lshlrev_b32 %0, 16, %0" : "+v"(a));
  if constexpr (K == 6) asm volatile("v_add_f32 %0, 0x3727c5ac, %0\n\tv_cvt_pk_bf16_f32 %0, %0, %0" : "+v"(a));
  if constexpr (K == 7) asm volatile("v_lshlrev_b32 %0, 16, %0\n\tv_sqrt_f32 %0, %0" : "+v"(a));
  if constexpr (K == 8) asm volatile("v_cvt_pk_bf16_f32 %0, %2, %3\n\tv_and_b32 %1, 0xffff0000, %0\n\tv_rcp_f32 %1, %1" : "=&v"(ms), "=&v"(c) : "v"(b), "v"(a));
  if constexpr (K == 9) asm volatile("v_mul_f32 %0, 0xbfb8aa3b, %1" : "=v"(c1) : "v"(c));
}
template <int ST> DI void dpp_step(float& v) {
  if constexpr (ST == 0) asm volatile("v_add_f32_dpp %0, %0, %0 quad_perm:[1,0,3,2] row_mask:0xf bank_mask:0xf" : "+v"(v));
  if constexpr (ST == 1) asm volatile("v_add_f32_dpp %0, %0, %0 quad_perm:[2,3,0,1] row_mask:0xf bank_mask:0xf" : "+v"(v));
  if constexpr (ST == 2) asm volatile("v_add_f32_dpp %0, %0, %0 row_half_mirror row_mask:0xf bank_mask:0xf" : "+v"(v));
  if constexpr (ST == 3) asm volatile("v_add_f32_dpp %0, %0, %0 row_mirror row_mask:0xf bank_mask:0xf" : "+v"(v));
}
static_assert(D_ == 640, "");
template <class T> DI T spin_(T v) { asm volatile("" : "+s"(v)); return v; }
DI float fexp(float x) { return __builtin_amdgcn_exp2f(x * 1.44269504088896341f); }

#define TILER_SLEEP 3
#define MAPL 1
#define WP2 WP
#define TCHK_Q 10
#define WP 16
constexpr int wpos(int k) { return 3 * ((k * 24 + 12) / WP) + 2; }
constexpr int wslot(int q) { for (int k = 0; k < WP; k++) if (wpos(k) == q) return k; return -1; }
constexpr int wcount(int lo, int hi) { int n = 0; for (int k = 0; k < WP; k++) n += wpos(k) >= lo && wpos(k) < hi; return n; }
constexpr int NW_CONV = 2 * WP + WP2 - wcount(0, TCHK_Q);
static_assert(NW_CONV <= 60 && NW_CONV >= 24, "");
#define T1POLL_Q 12
constexpr int t1chk() { for (int q = 0; q < 72; q++) if (wslot(q) >= 0 && WP + wslot(q) - wcount(0, TCHK_Q) >= 30) return q; return 71; }
constexpr int T1CHK_Q = t1chk();
#define XT_STR 16384
#define LN_FIRST 0
#define OUT_AUX 16
#define PF2_G 14
#define PF2_FROM 0x7000
#define PFS 16
#define PFN (352 / PFS)
#define TOUCH (role6 != 0)
#define PAD1_AT 2
#define PAD2_AT 3
#define FIN_Q 18
#define SIG0_Q 62

#define ODD_SLEEP 1
#define CS_A 6
#define NPRE 8
#define POLL_G 11
#define CHECK_G 18
#define LANDA_G 31
#define LAND3_G 39
#define STA_G 46
#define STB_G 52
#define EPI_CREDIT 20
#define CREDIT_MAX 40
#define VOFF_V voff
#define VOFF_H voff
#define C16 16u
#define FINA_G 97
#define EPIA_G 56
#define SIGA_G 63
#define POLLA_G 68
#define CHKA_G 75
#define SIGB_G 76
#define POLLB_G 83
#define CHKB_G 86
#define SIGC_G 84
#define POLLC_G 90
#define CHKC_G 93
#define RATE1 5
#define RATE2 12
#define V_FROM_B0 0
#ifndef NVV
#define NVV 12
#endif
constexpr int g1_half(int g) { return g < 16 ? 0 : g < 32 ? 1 : g < 56 ? 0 : 1; }
constexpr int g1_m(int g) { return g < 56 ? g & 3 : 2 * (g & 1) + (g >= 68); }
constexpr int g1_ks(int g) { return g < 32 ? (g >> 1) & 6 : g < 56 ? 8 + 2 * ((g - 32) >> 2) : 8 + 2 * (((g - 56) % 12) >> 1); }
constexpr int hpos(int h, int k) { return k < 8 ? k + 8 * h : k + 8 + 12 * h; }
constexpr int g2_m(int g) { return g < 12 ? g & 1 : 2 + (g >= 18); }
constexpr int g2_ks(int g) { return g < 12 ? g & ~1 : 2 * ((g - 12) % 6); }
constexpr int slot0(int g) { return g <= 80 ? 2 * g : 160 + 3 * (g - 80); }
constexpr int SA_G = LANDA_G + 4, SB_G = LAND3_G + 4;
constexpr int NVE = V_FROM_B0 ? NVV : 0;
constexpr bool v_in_vgpr(int i) { return i < NVE || i >= NVE + 28; }
enum { OP_NONE, OP_DMA, OP_W, OP_V, OP_POLL, OP_TAB, OP_SIG, OP_H, OP_PUB, OP_ST, OP_CS, OP_PF };
struct Sched {
  int kind[232], arg[232];
  int cnt[233];
  constexpr int idx(int k, int a) const { if (k == OP_DMA && a < NPRE) return a; for (int p = 0; p < 232; p++) if (kind[p] == k && arg[p] == a) return cnt[p]; return 1000; }
  constexpr int wait(int g, int k, int a, int less = 0) const { const int n = cnt[slot0(g)] - less - 1 - idx(k, a); return n > 63 ? 63 : n; }
};
constexpr Sched make_sched() {
  Sched s{};
  auto put = [&](int p, int k, int a) { while (s.kind[p] != OP_NONE) p++; s.kind[p] = k; s.arg[p] = a; };
  put(slot0(POLL_G), OP_POLL, 0); put(slot0(POLLA_G), OP_POLL, 1); put(slot0(POLLB_G), OP_POLL, 2); put(slot0(POLLC_G), OP_POLL, 3);
  put(slot0(SIGA_G), OP_SIG, 0); put(slot0(SIGB_G), OP_SIG, 1); put(slot0(SIGC_G), OP_SIG, 2);
  put(slot0(SA_G + 2) + 1, OP_PUB, 0); put(slot0(SB_G + 2) + 1, OP_PUB, 1);
  for (int k = 0; k < 4; k++) { put(slot0(STA_G) + 2 * k + 1, OP_ST, k); put(slot0(STB_G) + 2 * k + 1, OP_ST, 4 + k); }
  for (int k = 0; k < 6; k++) put(2 * k + 21, OP_TAB, k);
  for (int k = 0; k < 3; k++) put(2 * k + 9, OP_CS, k);
  if (PF2_G >= 0) put(slot0(PF2_G), OP_PF, 0);
  int nw = NW_CONV, nd = NPRE, nv = 0, nh = 0, credit = 24;
  for (int g = 0; g < 104; g++) {
    credit += g < 80 ? RATE1 : RATE2;
    if (g == EPIA_G) credit += 2 * EPI_CREDIT;
    if (g == 68 || g == 80) credit += EPI_CREDIT;
    if (credit > CREDIT_MAX) credit = CREDIT_MAX;
    for (int p = slot0(g); p < slot0(g + 1); p++) {
      if (s.kind[p] != OP_NONE || credit < 4) continue;
      const int vcap = (g < 32 ? NVE : NVV) + 4 + (g < 20 ? 0 : g < 32 ? 6 * ((g - 16) >> 2) : 24) + (g >= EPIA_G ? 12 : 0) + (g >= 68 ? 60 : 0);
      const int hcap = (g < 70 ? 0 : g < 80 ? 6 * ((g - 68) >> 1) : 36 + 2 * (g - 80));
      const int hok = g < CHKA_G ? 0 : g < CHKB_G ? 24 : g < CHKC_G ? 36 : 48;
      const bool vfirst = nv < 60 && g >= 80 - 8 && nv / 5 <= nh / 2 + 2;
      if ((g >= CHECK_G || nd < 16) && nd < 40) { s.kind[p] = OP_DMA; s.arg[p] = nd++; }
      else if (nw < 60) { s.kind[p] = OP_W; s.arg[p] = nw++; }
      else if (!vfirst && nh < hok && nh < hcap) { s.kind[p] = OP_H; s.arg[p] = nh++; }
      else if (nv < 60 && nv < vcap && (V_FROM_B0 || g >= CHECK_G)) { s.kind[p] = OP_V; s.arg[p] = nv++; }
      else continue;
      credit -= 4;
    }
  }
  int c = NPRE;
  for (int p = 0; p <= 232; p++) {
    if (p == slot0(EPIA_G)) c += 3;
    if (p == slot0(68) || p == slot0(80) || p == slot0(FINA_G)) c += 2;
    s.cnt[p] = c;
    if (p < 232) c += s.kind[p] != OP_NONE;
  }
  return s;
}
constexpr Sched SCH = make_sched();

__global__ __launch_bounds__(256, 1) void fused_kernel(const u16* __restrict__ x, const u16* __restrict__ lar, const u16* __restrict__ aim,
    const u16* __restrict__ cre, const u16* __restrict__ cim, const u16* __restrict__ ldt, const u16* __restrict__ dsk,
    const u16* __restrict__ w1, const u16* __restrict__ w3, const u16* __restrict__ w2, u16* __restrict__ out, char* __restrict__ ws_arg) {
  extern __shared__ __attribute__((aligned(16))) char smem[];
  LAS char* const S = (LAS char*)smem;
  const int tid = threadIdx.x, lane = tid & 63, w = __builtin_amdgcn_readfirstlane(tid >> 6);
  const int b = blockIdx.x, xcd = b & 7, slot = b >> 3;
  STAMP_AT(0);
  u32 cpf;
  u64 pc;
  u32 len;
  asm volatile("s_getpc_b64 %0\n.Lfk_begin:\n\ts_mov_b32 %1, .Lfk_end-.Lfk_begin" : "=s"(pc), "=s"(len));
  const rsrc_t rc = __builtin_amdgcn_make_buffer_rsrc((void*)pc, 0, len, 0x00020000);
  {
    fresh(cpf);
    bld32<0>(cpf, rc, (tid < PFN ? tid : 0x10000) * (128 * PFS), (slot & (PFS - 1)) * 128);
    SCHED();
  }
  const int li = lane & 15, lg = lane >> 4;

  const int pr = xcd * 16 + (slot >> 1), odd = slot & 1;
  const int ch0 = 5 * pr + (odd ? 3 : 0), nch = odd ? 2 : 3;
  const int gr = (xcd >> MAPL) * (2 << MAPL) + (slot >> (4 - MAPL)), gj = (xcd & ((1 << MAPL) - 1)) * (16 >> MAPL) + (slot & ((16 >> MAPL) - 1));
  const int j2 = gj >> 1, sub = gj & 1;
  const int rg = tid;

  f32x2 xf[3][4];
  u32 f0;
  u32x4 W[60];
  u32 tag;
  u32x4 gi[5];
  const int role_ = w < nch ? 0 : (odd ? 2 : 3);
  int role1 = role_, role2 = role_, role3 = role_;
  asm volatile("" : "+s"(role1), "+s"(role2), "+s"(role3));
  u32x4 ld[16];
  u32 pm[10];
  sfor<0, 16>([&](auto I) { fresh(ld[I]); });
  sfor<0, 10>([&](auto I) { fresh(pm[I]); });
  fresh(tag);
  sfor<0, 5>([&](auto I) { fresh(gi[I]); });
  const u32 voff = opaque(lane * 16);
  const int part = lane & 7;
  const int tp = pr < 120 ? pr : 0, tg = (tp * 205) >> 10, tk = 2 * (tp - 5 * tg) + (w == 2 ? 10 : 0), tid2 = pr + (w == 2 ? 128 : 0);
  const u32 vr = opaque((lane >> 3) * (D_ * 2) + (part ^ ((lane >> 2) & 4)) * 16), vr2 = opaque(vr + 8 * D_ * 2);
  const u32 xso = spin_(pr * (16 * D_ * 2));
  const u32 pch = w < nch ? ch0 + w : ch0;
  const u32 pv1 = opaque(pch * 2), pv4 = opaque(pch * (N_ * 2) + lane * 4), pv2 = opaque(pch * (N_ * 2) + 256 + lane * 2), epo = opaque(WS_EPOCH), gvo = opaque(w * 5120 + 2048 + lane * 16);
  SCHED();
  asm volatile("" ::"s"(x), "s"(lar), "s"(aim), "s"(cre), "s"(cim), "s"(ldt), "s"(dsk), "s"(w1), "s"(w3), "s"(w2), "s"(out), "s"(ws_arg));
  char* ws;
  asm volatile("s_mov_b64 %0, %1" : "=s"(ws) : "s"(ws_arg));
  const rsrc_t rws = __builtin_amdgcn_make_buffer_rsrc(ws, 0, WS_BYTES, 0x00020000);
  if (role1 == 0) {
    RB(0, 1);
    gld16(pm[0], ldt, pv1);
    gld32(pm[1], lar, pv4); gld16(pm[2], lar, pv2);
    gld32(pm[3], aim, pv4); gld16(pm[4], aim, pv2);
    gld32(pm[5], cre, pv4); gld16(pm[6], cre, pv2);
    gld32(pm[7], cim, pv4); gld16(pm[8], cim, pv2);
    gld16(pm[9], dsk, pv1);
    gld32_sc1(tag, ws, epo);
    gtab_ld<1>(gi, gvo, odd ^ 1);
    RB(0, 1);
  }
  RJ(0, 1);
  if (role2 == 3) {
    RB(3, 2);
    const rsrc_t rx = __builtin_amdgcn_make_buffer_rsrc((void*)x, 0, L_ * D_ * 2, 0x00020000);
    sfor<0, 20>([&](auto I) { constexpr int h = I / 10, k = I % 10; dma_x<(h ? LD_U : LD_TS + 32) + k * 1024 - k * 128, k * 128>(h ? vr2 : vr, rx, xso); });
    gld32_sc1(tag, ws, epo);
  }
  RJ(3, 2);
  if (role3 == 2) {
    RB(2, 3);
    if (w == 2) __builtin_amdgcn_s_sleep(TILER_SLEEP);
  u32 wv[8];
  {
    const int l3 = tk >> 4, dl = (5 * ((lane >> 3) << l3) + 1) >> 1, d0 = (80 << l3) * (tk & 7) + (tk >> 3);
    const u32 ws_ = pr < 120 ? (20 * F_ * 2) << l3 : 0;
    wv[0] = opaque(pr < 120 ? ((d0 + dl) * F_ + 64 * tg + 8 * part) * 2 : 0);
#pragma unroll
    for (int i = 1; i < 8; i++) wv[i] = wv[0] + i * ws_;
    sfor<1, 8>([&](auto I) { tie(wv[I]); });
  }
    sfor<0, 8>([&](auto I) { gld128w(ld[2 * I], w1, wv[I]); gld128w(ld[2 * I + 1], w3, wv[I]); });
    gld32_sc1(tag, ws, epo);
  }
  RJ(2, 3);
  int role = role3, role4 = role3, role5 = role3, role6 = role3, role7 = role3, role8 = role3, od1 = odd, od2 = odd;
  asm volatile("" : "+s"(role), "+s"(role4), "+s"(role5), "+s"(role6), "+s"(role7), "+s"(role8), "+s"(od1), "+s"(od2));
  SCHED();
  STAMP_AT(25);
  LAS char *uown, *xown, *xown2, *hb, *gb;
  u32 tfo;
  const char* const wp = ws + WS_W13 + (gj * 4 + w) * 61440;
  u32 st2o, sig3, hoff, rsx, rto, hto[3], htl, addo, pollo, cso, li16, pubv, hid0, hidA, hidB, hidC, p3, l4, outo, outv;
  LAS char *pxr, *stwr, *strd, *redw, *redv, *bp[3][4], *sb[3];
  auto setup = [&](auto K_) {
    constexpr int k = K_;
    if constexpr (k == 0) pxr = opaque(S + LD_PX + w * 8320 + (lane >> 5) * 1040 + ((lane >> 1) & 1) * 528);
    if constexpr (k == 1) pxr = opaque(pxr + ((lane >> 2) & 1) * 256 + ((lane >> 3) & 3) * 64 + (lane & 1) * 32);
    if constexpr (k == 2) htl = opaque(WS_HT + (rg >> 4) * 163840 + (rg & 7) * 16);
    if constexpr (k >= 3 && k < 6) {
      constexpr int c = k - 3;
      const int kap = c < 2 ? 256 * c + 2 * pr + odd : (odd ? 257 + 2 * pr : 512 + pr);
      hto[c] = opaque((htl ^ (((kap >> 1) & 3) * 32)) + ((rg >> 3) & 1) * ((c ? 12 : 8) * 4096) + (((kap >> 5) + (c ? 8 : 0)) * 4096 + (kap & 31) * 128));
    }
    if constexpr (k == 6) addo = opaque(WS_CTRL + ((2 * w + (lane & 1)) * 8 + (slot & 7)) * 128);
    if constexpr (k == 7) pollo = opaque(WS_CTRL + (xcd * 8 + (lane & 7)) * 128);
    if constexpr (k == 8) cso = opaque(li < 10 ? li * 64 + lg * 16 : 0x40000000);
    if constexpr (k == 9) { st2o = spin_(WS_ST2 + (gr * 4 + (gj >> 2)) * 2048); sig3 = spin_(WS_CTRL + (192 + (gr * 4 + (gj >> 2)) * 4) * 128); }
    if constexpr (k == 10) hoff = spin_(WS_HID + ((8 * gr + 2 * sub) * 48 + 12 * w) * 1024);
    if constexpr (k == 11) { rsx = spin_((2 * sub + (w >> 1)) * 32); rto = spin_((j2 * 2 + (w & 1)) * 1536); }
    if constexpr (k == 12) { li16 = opaque(li * 16); l4 = opaque(lane * 4); }
    if constexpr (k == 13) strd = opaque(S + (j2 == 0 ? 4096 : 0) + li * 32);
    if constexpr (k == 14) stwr = opaque(strd + ((li >> 2) == lg ? w * 8 : 2048));
    if constexpr (k == 15) pubv = opaque(st2o + (gj & 3) * 256 + (tid < 16 ? tid * 16 : 0x40000000));
    if constexpr (k == 16) hid0 = opaque(WS_HID + (8 * gr * 48 + 3 * gj) * 1024 + w * 256 + li16);
    if constexpr (k == 17) hidA = opaque(hid0 + lg * 49152);
    if constexpr (k == 18) hid0 = opaque(hid0 + (4 + 2 * (lg & 1)) * 49152);
    if constexpr (k == 19) { hidB = opaque(hid0 + (lg >> 1) * 1024); hidC = opaque(hid0 + 2048 + (lg >> 1) * 0x40000000); }
    if constexpr (k == 20) p3 = opaque(WS_CTRL + (192 + (gr * 4 + w) * 4) * 128);
    if constexpr (k == 21) bp[0][0] = opaque(S + (lane >> 2) * 128 + (((lane >> 3) & 3) * 32) + (lane & 3) * 8);
    if constexpr (k == 22) sb[0] = opaque((LAS char*)(size_t)(((u32)(size_t)bp[0][0] ^ ((gj & 3) * 32)) + w * 4096));
    if constexpr (k == 23) {
      redw = opaque(S + (w >> 1) * 25600 + (li & 3) * 272 + (li >> 2) * 64);
      redv = opaque(redw + 5120 + lg * 16);
      redw = opaque(redw + (2 * (w & 1) + (lg & 1)) * 1280 + (lg >> 1) * 32);
      outo = opaque(((128 * gr + 16 * (2 * sub + (w >> 1)) + li) * D_ + 80 * j2) * 2);
      outv = opaque(outo + 128 + 8 * lg + (w & 1) * 0x40000000);
      outo = opaque(outo + 64 * (w & 1) + 32 * (lg & 1) + 16 * (lg >> 1));
    }
  };
  auto setup1 = [&]() __attribute__((always_inline)) {
    uown = opaque(S + LD_U + (rg & 1) * U_SZ + ((rg >> 1) + 16) * 16);
    xown = opaque(S + LD_TS + rg * 16);
    xown2 = opaque(xown + (odd ? 4096 : 8192));
    hb = opaque(S + LD_IMG + (li & 3) * IMG_CP + (4088 - 3584) - 32 * lg - 8 * (li >> 2) - 128 * w);
    gb = opaque(S + LD_U + (li - lg + 16) * 16 - 64 * w);
    tfo = opaque(WS_TFLAG + (5 * ((3 * gj) >> 1) + (lane < 10 ? lane : 0)) * 128);
    sfor<0, 9>(setup);
  };
  if (TOUCH) TOUCH_PAD();
  if (role8 != 3) setup1();
  SCHED();
  STAMP_AT(26);
  float dt, s1i, s2, s2i;
  f32x2 w1v[3], w256v[3];
  if (role == 0) {
    RB(0, 5);
    gtab_ld<0>(gi, gvo, odd);
    RB(0, 5);
    vmwait<11>();
    sfor<0, 5>([&](auto I) { tie(pm[I]); });
    const u32 ldtv = pm[0], la2 = pm[1], la1 = pm[2], ai2 = pm[3], ai1 = pm[4];
    STAMP_AT(27);
    dt = rbf(fexp(bflo(ldtv)));
    f32x2 w8v[3];
#pragma unroll
    for (int m = 0; m < 3; m++) {
      const float la = m == 0 ? bflo(la2) : (m == 1 ? bfhi(la2) : bflo(la1));
      const float ai = m == 0 ? bflo(ai2) : (m == 1 ? bfhi(ai2) : bflo(ai1));
      const float a = -rbf(fexp(la)) * dt, bb = ai * dt;
      w8v[m] = cexp(8.f * a, 8.f * bb);
      w1v[m] = cexp(a, -bb);
      w256v[m] = cexp(256.f * a, -256.f * bb);
    }
    PIN(w8v[0]); PIN(w8v[1]); PIN(w8v[2]); PIN(w1v[0]); PIN(w1v[1]); PIN(w1v[2]); PIN(w256v[0]); PIN(w256v[1]); PIN(w256v[2]);
    STAMP_AT(14);
    set_m0n<0>(w * TL_SZ);
    {
      f32x2 ws8[3], v[3];
      u32 pk[4][3];
#pragma unroll
      for (int m = 0; m < 3; m++) { ws8[m] = f32x2{-w8v[m].y, w8v[m].x}; v[m] = w8v[m]; pk[0][m] = 0x00003c00u; pk[1][m] = pkh2(v[m]); }
      sfor<0, 8>([&](auto G_) {
        constexpr int g = G_;
#pragma unroll
        for (int i = (g == 0 ? 2 : 0); i < 4; i++)
#pragma unroll
          for (int m = 0; m < 3; m++) { v[m] = cmul(v[m], w8v[m], ws8[m]); pk[i][m] = pkh2(v[m]); }
        constexpr int r0_ = 4 * g;
        put12<r0_ * TROW, (r0_ + 1) * TROW, (r0_ + 2) * TROW, (r0_ + 3) * TROW>(pk);
        if constexpr (g == PAD1_AT) { SCHED(); PAGE_PAD(1, .Lpad2); SCHED(); }
      });
    }
    STAMP_AT(28);
    vmwait<5>();
    sfor<5, 10>([&](auto I) { tie(pm[I]); });
    tie(tag);
    tag += 1u;
    const u32 dsv = pm[9];
    const u32 cr2 = pm[5], cr1 = pm[6], ci2 = pm[7], ci1 = pm[8];
    f32x2 cc[3];
    u32 cm = cr2 & 0x7fffu;
    { u32 t;
      t = (cr2 >> 16) & 0x7fffu; cm = cm > t ? cm : t;
      t = ci2 & 0x7fffu; cm = cm > t ? cm : t;
      t = (ci2 >> 16) & 0x7fffu; cm = cm > t ? cm : t;
      t = cr1 & 0x7fffu; cm = cm > t ? cm : t;
      t = ci1 & 0x7fffu; cm = cm > t ? cm : t; }
    cm = dpp_max<0xB1>(cm); cm = dpp_max<0x4E>(cm); cm = dpp_max<0x141>(cm); cm = dpp_max<0x140>(cm);
    { u32 a0 = __builtin_amdgcn_readlane(cm, 0), a1 = __builtin_amdgcn_readlane(cm, 16), a2 = __builtin_amdgcn_readlane(cm, 32), a3 = __builtin_amdgcn_readlane(cm, 48);
      a0 = a0 > a1 ? a0 : a1; a2 = a2 > a3 ? a2 : a3; cm = a0 > a2 ? a0 : a2; }
    int ec = (int)(cm >> 7);
    ec = ec < 16 ? 16 : (ec > 254 ? 254 : ec);
    const float s1 = asf((u32)(127 + 13 + 127 - ec) << 23);
    s1i = asf((u32)(ec - 13) << 23);
    const float bound = 544.f * dt * bflo(cm) + fabsf(bflo(dsv));
    int eb = (int)(asu(bound) >> 23);
    eb = eb < 16 ? 16 : (eb > 254 ? 254 : eb);
    s2 = asf((u32)(127 + 14 + 127 - eb) << 23);
    s2i = asf((u32)(eb - 14) << 23);
    cc[0] = f32x2{bflo(cr2) * s1, -bflo(ci2) * s1}; cc[1] = f32x2{bfhi(cr2) * s1, -bfhi(ci2) * s1}; cc[2] = f32x2{bflo(cr1) * s1, -bflo(ci1) * s1};
    set_m0n<TH_M0>(w * TH_SZ);
    constexpr int HO = LD_TH - TH_M0 - 5 * TROW;
    {
      f32x2 ws1[3], ws256[3], v[3], k2[3], k4[3], k6[3];
      u32 pk[4][3];
#pragma unroll
      for (int m = 0; m < 3; m++) { ws1[m] = f32x2{-w1v[m].y, w1v[m].x}; ws256[m] = f32x2{-w256v[m].y, w256v[m].x}; v[m] = cc[m]; pk[0][m] = pkh2(v[m]); }
      put3<HO + 25 * TROW>(pk[0]);
#pragma unroll
      for (int i = 0; i < 4; i++)
#pragma unroll
        for (int m = 0; m < 3; m++) { v[m] = cmul(v[m], w1v[m], ws1[m]); pk[i][m] = pkh2(v[m]); if (i == 1) k2[m] = v[m]; if (i == 3) k4[m] = v[m]; }
      put12<HO + 5 * TROW, HO + 6 * TROW, HO + 7 * TROW, HO + 8 * TROW>(pk);
      if constexpr (PAD2_AT == 1) { SCHED(); PAGE_PAD(2, .Lpad_ret); SCHED(); }
#pragma unroll
      for (int i = 0; i < 4; i++)
#pragma unroll
        for (int m = 0; m < 3; m++) { v[m] = cmul(v[m], w1v[m], ws1[m]); pk[i][m] = pkh2(v[m]); if (i == 1) k6[m] = v[m]; }
      put12<HO + 9 * TROW, HO + 10 * TROW, HO + 11 * TROW, HO + 12 * TROW>(pk);
      if constexpr (PAD2_AT == 2) { SCHED(); PAGE_PAD(2, .Lpad_ret); SCHED(); }
#pragma unroll
      for (int m = 0; m < 3; m++) {
        k2[m] = cmul(k2[m], w256v[m], ws256[m]); pk[0][m] = pkh2(k2[m]);
        k4[m] = cmul(k4[m], w256v[m], ws256[m]); pk[1][m] = pkh2(k4[m]);
        k6[m] = cmul(k6[m], w256v[m], ws256[m]); pk[2][m] = pkh2(k6[m]);
        v[m] = cmul(v[m], w256v[m], ws256[m]); pk[3][m] = pkh2(v[m]);
      }
      put12<HO + 13 * TROW, HO + 14 * TROW, HO + 15 * TROW, HO + 16 * TROW>(pk);
      if constexpr (PAD2_AT == 3) { SCHED(); PAGE_PAD(2, .Lpad_ret); SCHED(); }
#pragma unroll
      for (int i = 0; i < 2; i++)
#pragma unroll
        for (int m = 0; m < 3; m++) {
          k4[m] = cmul(k4[m], w256v[m], ws256[m]); pk[2 * i][m] = pkh2(k4[m]);
          v[m] = cmul(v[m], w256v[m], ws256[m]); pk[2 * i + 1][m] = pkh2(v[m]);
        }
      put12<HO + 17 * TROW, HO + 18 * TROW, HO + 19 * TROW, HO + 20 * TROW>(pk);
      if constexpr (PAD2_AT == 4) { SCHED(); PAGE_PAD(2, .Lpad_ret); SCHED(); }
#pragma unroll
      for (int i = 0; i < 4; i++)
#pragma unroll
        for (int m = 0; m < 3; m++) { v[m] = cmul(v[m], w256v[m], ws256[m]); pk[i][m] = pkh2(v[m]); }
      put12<HO + 21 * TROW, HO + 22 * TROW, HO + 23 * TROW, HO + 24 * TROW>(pk);
      if constexpr (PAD2_AT == 5) { SCHED(); PAGE_PAD(2, .Lpad_ret); SCHED(); }
    }
    STAMP_AT(15);
    const int rr = lane & 31, hf = lane >> 5;
    LAS char* const pa = S + LD_TH + w * TH_SZ + (rr > 20 ? rr - 16 : rr) * TROW + hf * 16;
    LAS char* const pb = S + w * TL_SZ + rr * TROW + hf * 16;
    f32x16 kacc;
    u32x4 fa[24], fb[24];
    sfor<0, 28>([&](auto K_) {
      constexpr int ks = K_;
      if constexpr (ks < 24) { fa[ks] = *(LAS u32x4*)(pa + ks * 32); fb[ks] = *(LAS u32x4*)(pb + ks * 32); }
      if constexpr (ks >= 4) mf_h32<ks == 4>(kacc, fa[ks - 4], fb[ks - 4]);
      SCHED();
    });
    asm volatile("s_nop 7\n\ts_nop 7" : "+v"(kacc));
    const float fac = 2.f * dt * s1i * s2;
    LAS char* const kc = S + LD_KC + w * KC_SZ + rr * 56 + hf * 8;
    const float k8 = kacc[8] * fac + (hf ? bflo(dsv) * s2 : 0.f);
    const u32x2 q0 = {pkh(kacc[0] * fac, kacc[1] * fac), pkh(kacc[2] * fac, kacc[3] * fac)};
    const u32x2 q1 = {pkh(kacc[4] * fac, kacc[5] * fac), pkh(kacc[6] * fac, kacc[7] * fac)};
    const u32x2 q2 = {pkh(k8, hf ? 0.f : kacc[9] * fac), pkh(kacc[10] * fac, kacc[11] * fac)};
    *(LAS u32x2*)(kc) = q0; *(LAS u32x2*)(kc + 16) = q1; *(LAS u32x2*)(kc + 32) = q2;
    *(LAS float*)(S + LD_MISC + w * 4) = s2i;
    STAMP_AT(30);
    vmwait<0>();
    sfor<0, 5>([&](auto I) { tie(gi[I]); });
    u32 e[23];
#pragma unroll
    for (int k = 0; k < 3; k++) e[k] = *(LAS u16*)(size_t)(gi[0][k] + 4);
#pragma unroll
    for (int k = 0; k < 20; k++) e[3 + k] = *(LAS u16*)(size_t)(gi[k >> 2][k & 3]);
    LAS char* const imw = S + LD_IMG + w * IMG_SZ + lane * 16;
    if (lane < 8) *(LAS u32x4*)(S + LD_IMG + w * IMG_SZ + (lane >> 1) * IMG_CP + 4096 + (lane & 1) * 16) = u32x4{0, 0, 0, 0};
    SCHED();
    sfor<0, 4>([&](auto B_) {
      constexpr int bl = B_;
      u32x4 cp[4];
#pragma unroll
      for (int c = 0; c < 4; c++)
#pragma unroll
        for (int n = 0; n < 4; n++) cp[c][n] = __builtin_amdgcn_perm(e[gsel(bl, 4 - c + 2 * n)], e[gsel(bl, 3 - c + 2 * n)], 0x05040100u);
      SCHED();
#pragma unroll
      for (int c = 0; c < 4; c++) *(LAS u32x4*)(imw + c * IMG_CP + bl * 1024) = cp[c];
      SCHED();
    });
  }
  RJ(0, 5);
  PAGE_ALIGN(1);
  if (role4 == 3) {
    RB(3, 6);
    constexpr int XO = LD_TS + 32 - LD_U;
    LAS char* const p0 = opaque(S + LD_U + ((lane & 1) ? 0 : XO) + ((lane >> 2) & 3) * 128 + (lane & 8) * 8 + lg * 16 + (lane & 2) * 4);
    LAS char* const p1 = opaque((LAS char*)(size_t)((u32)(size_t)p0 + ((lane & 8) ? -64 : 64)));
    LAS char* const pl = opaque(S + LD_U + lane * 16);
    const int xo = opaque(WS_XT + (lane >> 3) * XT_STR + pr * 128 + (lane & 7) * 16);
    vmwait<1>();
    STAMP_AT(27);
    auto transpose = [&]() __attribute__((always_inline)) {
    sfor<0, 2>([&](auto H) {
      u32x4 fr[10];
#pragma unroll
      for (int k = 0; k < 10; k++) { LAS char* const p = ((k & 1) ? p1 : p0) + (5 * H + (k >> 1)) * 1024; fr[k] = trfrag(p, p + 512); }
#pragma unroll
      for (int k = 0; k < 10; k++) __builtin_amdgcn_raw_buffer_store_b128(fr[k], rws, xo + (10 * H + k) * (8 * XT_STR), 0, SC1);
      asm volatile("" ::: "memory");
      SCHED();
    });
    };
    __builtin_amdgcn_raw_buffer_store_b64(u32x2{0u, 0u}, rws, WS_CTRL + pr * 512 + lane * 8, 0, SC1);
    asm volatile("" ::: "memory");
    u32x4 xr[20];
#pragma unroll
    for (int i = 0; i < 20; i++) xr[i] = *(LAS u32x4*)(pl + (i < 10 ? XO : 0) + (i % 10) * 1024);
    STAMP_AT(14);
    u32 rec[2];
    sfor<0, 2>([&](auto HH) {
      constexpr int h = HH;
      if constexpr (h == LN_FIRST) { SCHED(); transpose(); }
      float sm = 0.f, sq = 0.f;
#pragma unroll
      for (int i = 0; i < 10; i++)
#pragma unroll
        for (int e = 0; e < 4; e++) { const u32 v = xr[10 * h + i][e]; sm = dot2(v, 0x3f803f80u, sm); sq = dot2(v, v, sq); }
      sm = dpp_add<0xB1>(sm); sm = dpp_add<0x4E>(sm); sm = dpp_add<0x141>(sm);
      sq = dpp_add<0xB1>(sq); sq = dpp_add<0x4E>(sq); sq = dpp_add<0x141>(sq);
      const float mean = sm * (1.f / D_);
      const float var = fmaxf(sq * (1.f / D_) - mean * mean, 0.f);
      rec[h] = opaque(pkbf(mean, __builtin_amdgcn_sqrtf(rbf(rbf(var) + 1e-5f))));
    });
    if constexpr (LN_FIRST == 2) transpose();
    const u32 rc1 = __builtin_amdgcn_ds_bpermute(((lane & 7) * 8 + (lane >> 3)) * 4, (lane & 1) ? rec[1] : rec[0]);
    PIN(rc1);
    STAMP_AT(28);
    asm volatile("s_waitcnt vmcnt(0)" ::"v"(rc1) : "memory");
    tie(tag);
    tag += 1u;
    if (lane < 16) __builtin_amdgcn_raw_buffer_store_b64(u32x2{rc1, tag}, rws, WS_ST1 + pr * 128 + lane * 8, 0, SC1);
    asm volatile("" ::: "memory");
    STAMP_AT(29);
  }
  RJ(3, 6);
  if (role5 == 2) {
    RB(2, 7);
    LAS char* const TS = S + LD_TS + (w == 2 ? 10368 : 0);
    LAS char* const pw = opaque(TS + part * 1056 + (lane >> 3) * 32);
    LAS char* const prd = opaque(TS + lane * 8);
    int go[2];
#pragma unroll
    for (int h = 0; h < 2; h++) { const int t3 = 2 * tg + h, tj = (t3 * 43) >> 7, t = t3 - 3 * tj; go[h] = opaque(lane * 16 + (pr < 120 ? WS_W13 + ((tj * 80 + tk) * 3 + t) * 1024 : 0x40000000)); }
    u32x4 fr[2][8];
    f32x4 csp;
    sfor<0, 2>([&](auto U) {
      constexpr int u = U;
      vmwait<9>();
      sfor<8 * u, 8 * u + 8>([&](auto I) { tie(ld[I]); });
      if constexpr (u == 0) STAMP_AT(27);
#pragma unroll
      for (int i = 0; i < 4; i++) {
        const u32x4 g = ld[8 * u + 2 * i], up = ld[8 * u + 2 * i + 1];
        *(LAS u32x4*)(pw + i * 256) = u32x4{g.x, up.x, g.y, up.y};
        *(LAS u32x4*)(pw + i * 256 + 16) = u32x4{g.z, up.z, g.w, up.w};
      }
#pragma unroll
      for (int f = 0; f < 8; f++) fr[u][f] = trfrag(prd + f * 1056, prd + f * 1056 + 512);
#pragma unroll
      for (int f = 0; f < 8; f++) __builtin_amdgcn_raw_buffer_store_b128(fr[u][f], rws, go[f >> 2] + ((f & 3) * 60 + u * 3) * 1024, 0, SC1);
      asm volatile("" ::: "memory");
    });
    SCHED();
    sfor<0, 8>([&](auto F) {
      const u32 o1 = li == F ? 0x3f803f80u : 0u;
      u32x4 sel = {o1, o1, o1, o1};
      asm volatile("s_nop 1" : "+v"(sel));
      mf_bfv<F == 0>(csp, fr[0][F], sel); mf_bfv<false>(csp, fr[1][F], sel);
    });
    {
      const int t3 = 2 * tg + (li >> 2), tj = (t3 * 43) >> 7, t = t3 - 3 * tj;
      asm volatile("s_nop 7" : "+v"(csp));
      __builtin_amdgcn_raw_buffer_store_b128(__builtin_bit_cast(u32x4, csp), rws, li < 8 && pr < 120 ? WS_CS + ((tj * 4 + (li & 3)) * 3 + t) * 640 + (tk >> 1) * 64 + lg * 16 : 0x40000000, 0, SC1);
      asm volatile("" ::: "memory");
    }
    STAMP_AT(14);
    vmwait<17>();
    tie(tag);
    tag += 1u;
    if (w == 2) {
      vmwait<1>();
      if (lane == 0) __builtin_amdgcn_raw_buffer_store_b32(tag, rws, WS_TFLAG + tid2 * 128, 0, SC1);
      const rsrc_t rw2 = __builtin_amdgcn_make_buffer_rsrc((void*)w2, 0, D_ * F_ * 2, 0x00020000);
      sfor<0, 5>([&](auto I) {
        const int id = lane + 64 * I, rho = (id * 6554) >> 16, c = id - 10 * rho;
        sfor<0, 3>([&](auto U) {
          set_m0n<2 * TL_SZ + U * 5120 + I * 1024>(0u);
          dma_nt((rho * D_ + 8 * c) * 2, rw2, (32 * ((3 * pr + U) >> 3) * D_ + 80 * ((3 * pr + U) & 7)) * 2);
        });
      });
    }
    STAMP_AT(28);
  }
  RJ(2, 7);
  if (role7 == 3) setup1();
  const int un = 3 * pr + (w < 3 ? w : 0), uj = un & 7, uk = un >> 3;
  if (w == 3) {
    *(LAS u32x4*)(S + LD_U + lg * U_SZ + li * 16) = u32x4{0, 0, 0, 0};
    if (lane < 32) *(LAS u32x4*)(S + LD_U + (4 + lg) * U_SZ + li * 16) = u32x4{0, 0, 0, 0};
    u32x4 st[16];
    if (S1_SLEEP0 && !odd) __builtin_amdgcn_s_sleep(S1_SLEEP0);
    for (u32 spins = 0;; spins++) {
      if (S1_SLEEP && spins) __builtin_amdgcn_s_sleep(S1_SLEEP);
      asm volatile("" ::: "memory");
#pragma unroll
      for (int i = 0; i < 16; i++) st[i] = __builtin_amdgcn_raw_buffer_load_b128(rws, voff + (i & 3) * 1024, WS_ST1 + (i >> 2) * 4096, SC1);
      u32 an = ~0u, orr = 0u;
#pragma unroll
      for (int i = 0; i < 16; i++) { an &= st[i].y & st[i].w; orr |= st[i].y | st[i].w; }
      if (odd && spins == 0) {
        asm volatile("s_waitcnt vmcnt(0)" ::"v"(an), "v"(orr) : "memory");
        if (lane == 0) __builtin_amdgcn_raw_buffer_store_b32(tag, rws, WS_TFLAG + tid2 * 128, 0, SC1);
        asm volatile("" ::: "memory");
      }
      if (__all(an == tag && orr == tag) || spins > SPIN_CAP) break;
    }
    asm volatile("" ::: "memory");
    STAMP_AT(30);
    const u32 l64 = lane * 64;
    auto xdma = [&](auto C) {
      const int ch = ch0 + (C < nch ? C : 1);
      const u32 so = WS_XT + (ch >> 2) * XT_STR + (ch & 3) * 16;
      sfor<0, 4>([&](auto Q) { set_m0n<LD_TS + C * 4096 + Q * 1024>(0u); dma_so(l64, rws, so + Q * 4096); });
    };
    xdma(IC<0>{});
    LAS char* const sp = S + LD_U + (2 + (lane & 2) + ((lane >> 2) & 1)) * U_SZ + ((lane >> 3) + 16) * 16 + (lane & 1) * 8;
#pragma unroll
    for (int i = 0; i < 16; i++) *(LAS u32x2*)(sp + i * 128) = u32x2{st[i].x, st[i].z};
    xdma(IC<1>{});
    if (2 < nch) xdma(IC<2>{});
  }
  asm volatile("" ::"v"(cpf));
  STAMP_AT(1);
  BARRIER();
  STAMP_AT(2);
  u32 tf, tf1;
  poll_ld<0>(tf, rws, tfo);
  const u32 m0w = w * 1024;
  const rsrc_t rht = __builtin_amdgcn_make_buffer_rsrc(ws + WS_HT + gr * 163840, 0, 163840, 0x00020000);
  u32x4 csl[3];
  {
    u32x2 Hl[2][8], Hh[2][8];
    u32x4 G[2][8];
    auto rd = [&](auto C_, auto HH_, auto N_) {
      constexpr int c = C_, hh = HH_, n = N_, i = n / 3, k = n % 3;
      if constexpr (k == 0) G[hh][i] = *(LAS u32x4*)(gb + (2 * c + hh) * U_SZ + 256 * i);
      else {
        const u32x2 v = *(volatile LAS u32x2*)(hb + c * IMG_SZ + 512 * (7 - i) + 16 * hh + (k == 2 ? 8 : 0));
        if constexpr (k == 1) Hl[hh][i] = v; else Hh[hh][i] = v;
      }
    };
    float inv[3];
    {
      const u32x4 sa = *(LAS u32x4*)(uown + 2 * U_SZ), sb = *(LAS u32x4*)(uown + 4 * U_SZ);
      sfor<0, 24>([&](auto N_) { if constexpr (N_ % 3 != 0) rd(IC<0>{}, IC<0>{}, N_); });
#pragma unroll
      for (int c = 0; c < 3; c++) inv[c] = *(LAS float*)(S + LD_MISC + (c == 2 && odd ? 1 : c) * 4);
      SCHED();
      f32x2 ra[4], rb[4];
#pragma unroll
      for (int i = 0; i < 4; i++) {
        const u32 q0 = i < 2 ? sa[2 * i] : sb[2 * i - 4], q1 = i < 2 ? sa[2 * i + 1] : sb[2 * i - 3];
        ra[i] = f32x2{__builtin_amdgcn_rcpf(bfhi(q0)), __builtin_amdgcn_rcpf(bfhi(q1))};
        rb[i] = -f32x2{bflo(q0), bflo(q1)} * ra[i];
        tie(ra[i]); tie(rb[i]);
      }
      auto make_u = [&](auto C, u32x4 xc) {
        u32x4 v;
#pragma unroll
        for (int i = 0; i < 4; i++) { xf[C][i] = f32x2{bflo(xc[i]), bfhi(xc[i])}; v[i] = pkh2(__builtin_elementwise_fma(xf[C][i], ra[i], rb[i])); }
        *(LAS u32x4*)(uown + 2 * C * U_SZ) = v;
      };
      if (w == 3) vmwait<5>();
      __builtin_amdgcn_s_barrier();
      asm volatile("" ::: "memory");
      make_u(IC<0>{}, *(LAS u32x4*)(xown));
      if (w == 3) vmwait<1>();
      BARRIER();
      STAMP_AT(5);
      const u32x4 x1 = *(LAS u32x4*)(xown + 4096), x2 = *(LAS u32x4*)(xown2);
      sfor<0, 8>([&](auto I) { rd(IC<0>{}, IC<0>{}, IC<3 * I>{}); });
      SCHED();
      make_u(IC<1>{}, x1); make_u(IC<2>{}, x2);
      SCHED();
    }
    auto w2_tile = [&]() __attribute__((always_inline)) {
      LAS char* const pr0 = S + 2 * TL_SZ + (w < 3 ? w : 0) * 5120 + (8 * lg + ((lane >> 2) & 3)) * 160;
      u32x4 fr[5];
#pragma unroll
      for (int t = 0; t < 5; t++) {
        const int col = 16 * t + 4 * (lane & 3);
        fr[t] = trfrag(pr0 + col * 2, pr0 + 640 + col * 2);
      }
      if (w < 3) {
#pragma unroll
        for (int t = 0; t < 5; t++) __builtin_amdgcn_raw_buffer_store_b128(fr[t], rws, lane * 16 + WS_W2 + ((uj * 48 + uk) * 5 + t) * 1024, 0, SC1);
      }
      asm volatile("" ::: "memory");
    };
    STAMP_AT(19);
    constexpr int PX_M0 = 45056;
    set_m0n<PX_M0>(w * 2080);
    auto putreg = [&](auto A_, auto R_, const f32x4& v) { constexpr int al = A_, r = R_; put1<LD_PX - PX_M0 + (al >> 1) * 8320 + (al & 1) * 1040 + (r & 1) * 528 + (r >> 1) * 256>(v[r]); };
    auto putacc = [&](const f32x4& v) { sfor<0, 4>([&](auto R) { putreg(IC<7>{}, R, v); }); };
    auto fin_math = [&](auto C_, const f32x4 (&pv)[2][4]) {
      constexpr int c = C_;
      SCHED();
      u32x4 o;
#pragma unroll
      for (int t = 0; t < 2; t++) {
        const f32x4 y = (pv[t][0] + pv[t][1]) + (pv[t][2] + pv[t][3]);
        const f32x2 iv = {inv[c], inv[c]};
        const f32x2 ha = __builtin_elementwise_fma(y.xy, iv, xf[c][2 * t]), hb2 = __builtin_elementwise_fma(y.zw, iv, xf[c][2 * t + 1]);
        o[2 * t] = pkbf(ha.x, ha.y); o[2 * t + 1] = pkbf(hb2.x, hb2.y);
      }
      __builtin_amdgcn_raw_buffer_store_b128(o, rws, hto[c], 0, SC1);
      asm volatile("" ::: "memory");
      SCHED();
    };
    f32x4 last7;
    auto channel = [&](auto C_) {
      constexpr int c = C_;
      f32x4 acc[8], pv[2][4];
      sfor<0, 72>([&](auto Q_) {
        constexpr int q = Q_, hh = q / 36, n = q % 36;
        if constexpr (n == 0) {
          lgkwait<(hh == 0) ? 15 : 9>();
          if constexpr (hh == 1) {
            __builtin_amdgcn_s_barrier();
          }
          SCHED();
        }
        constexpr int al = n < 1 ? 0 : n < 3 ? 1 : n < 6 ? 2 : n < 10 ? 3 : n < 15 ? 4 : n < 21 ? 5 : n < 28 ? 6 : 7;
        constexpr int m1 = n - al * (al + 1) / 2;
        mf_hv<hh == 0 && m1 == 0>(acc[al], G[hh][al - m1], u32x4{Hl[hh][m1].x, Hl[hh][m1].y, Hh[hh][m1].x, Hh[hh][m1].y});
        if constexpr (n % 3 != 2) {
          constexpr int r = n - n / 3;
          if constexpr (hh == 0) rd(C_, IC<1>{}, IC<r>{});
          else if constexpr (c < 2) rd(IC<c + 1>{}, IC<0>{}, IC<r>{});
        }
        if constexpr (c == 0 && q == TCHK_Q) poll_spin<0, 0, true>(tf, rws, tfo, tag);
        if constexpr (c == 1 && q == T1POLL_Q) poll_ld<16384>(tf1, rws, tfo);
        if constexpr (c == 1 && q == T1CHK_Q) poll_spin<16384, wcount(T1POLL_Q, q) + (SIG0_Q >= T1POLL_Q && SIG0_Q < q) + (FIN_Q >= T1POLL_Q), true>(tf1, rws, tfo, tag);
        if constexpr (wslot(q) >= 0 && (c > 0 || q >= TCHK_Q) && (c < 2 || wslot(q) < WP2)) {
          constexpr int i = c * WP + wslot(q) - wcount(0, TCHK_Q);
          if constexpr (c < 2) LDW(W[i], voff, wp, i); else ldw_in<i>(W[i], voff, wp);
        }
        if constexpr (c == 1 && q == SIG0_Q) { vmwait<wcount(FIN_Q + 1, q + 1) + (T1POLL_Q > FIN_Q && T1POLL_Q <= q)>(); __builtin_amdgcn_s_barrier(); add_one<0>(rws, addo); }
        if constexpr (c > 0) {
          if constexpr (q == 4 || q == 5) { putreg(IC<7>{}, IC<2 * (q - 4)>{}, last7); putreg(IC<7>{}, IC<2 * (q - 4) + 1>{}, last7); }
          if constexpr (q == 8) { lgkwait<2>(); __builtin_amdgcn_s_barrier(); asm volatile("" ::: "memory"); }
          if constexpr (q >= 8 && q < 12) { pv[0][q - 8] = *(LAS f32x4*)(pxr + (q - 8) * 2080); pv[1][q - 8] = *(LAS f32x4*)(pxr + 16 + (q - 8) * 2080); }
          if constexpr (q == FIN_Q) fin_math(IC<c - 1>{}, pv);
        }
        if constexpr (hh == 1) {
          if constexpr (n >= 8 && n < 32) putreg(IC<(n - 8) / 4>{}, IC<(n - 8) % 4>{}, acc[(n - 8) / 4]);
          if constexpr (n >= 32) putreg(IC<6>{}, IC<n - 32>{}, acc[6]);
        }
        SCHED();
      });
      last7 = acc[7];
    };
    channel(IC<0>{});
    STAMP_AT(16);
    channel(IC<1>{});
    STAMP_AT(17);
    sfor<NW_CONV - WP2, NW_CONV>([&](auto I) { fresh_a(W[I]); });
    fresh(f0);
    if (od1 == 0) {
      EB(0, 11);
      channel(IC<2>{});
      asm volatile("s_nop 6" ::"v"(last7) : "memory");
      putacc(last7);
    }
    EJ(0, 11);
    if (od2 != 0) {
      EB(1, 12);
      asm volatile("s_nop 6" ::"v"(last7) : "memory");
      putacc(last7);
      sfor<NW_CONV - WP2, NW_CONV>([&](auto I) { ldw_in<I>(W[I], voff, wp); });
      w2_tile();
    }
    EJ(1, 12);
    if (w == 0) poll_in<0>(f0, rws, pollo);
    BARRIER();
    STAMP_AT(6);
    {
      f32x4 pv[2][4];
#pragma unroll
      for (int v = 0; v < 4; v++) { pv[0][v] = *(LAS f32x4*)(pxr + v * 2080); pv[1][v] = *(LAS f32x4*)(pxr + 16 + v * 2080); }
      fin_math(IC<2>{}, pv);
    }
    STAMP_AT(7);
  }
  f32x4 acc1[2][4][3], sq[2], su[2], cs[3], acc2[4][5];
  u32x4 V[60], Hf[48], rec[2][4];
  u32x2 res[6];
  {
    const u32 z0 = opaque(0u);
    const u32 c16 = opaque(C16), c32 = opaque(32u);
    const char* const vp = ws + WS_W2 + (j2 * 4 + w) * 61440;
    const rsrc_t rrt = __builtin_amdgcn_make_buffer_rsrc((void*)&g_rtab, 0, sizeof(RTab), 0x00020000);
    const u32x4 on = opaque(u32x4{0x3f803f80u, 0x3f803f80u, 0x3f803f80u, 0x3f803f80u});
    u32 reso[6], fl[4];
    sfor<9, 24>(setup);
    sfor<1, 15>([&](auto I) {
      constexpr int i = I;
      if constexpr (i < 4) bp[0][i] = opaque((LAS char*)(size_t)((u32)(size_t)bp[0][0] ^ (i * 32)));
      else if constexpr (i < 12) bp[i >> 2][i & 3] = opaque(bp[0][i & 3] + (i >> 2) * 65536);
      else if constexpr (i > 12) sb[i - 12] = opaque(sb[0] + (i - 12) * 65536);
    });
    if (w == 0) poll_spin<0, 1, false>(f0, rws, pollo, 32u);
    __builtin_amdgcn_s_barrier();
    STAMP_AT(8);
    sfor<0, NPRE>([&](auto I) { set_m0n<I * 4096>(m0w); dma_m0(voff, rht); });
    vmwait<NPRE>();
    __builtin_amdgcn_s_barrier();
    add_one<8192>(rws, addo);
    u32x2 Bl[10], Bh[10];
    auto rdb = [&](auto F_, auto H_) {
      if constexpr (F_ < 160) {
        constexpr int f = F_, h = H_, g = f >> 1, o = hpos(g1_half(g), g1_ks(g) + (f & 1)) * 4096 + h * 2048;
        const u32x2 v = tr64(bp[o >> 16][g1_m(g)] + (o & 65535));
        if constexpr (h == 0) Bl[f % 10] = v; else Bh[f % 10] = v;
      }
    };
    u32x4 Sf[10];
    f32x4 pa[2];
    constexpr int CSR_G = CHECK_G + 2;
    const u32 cssb = spin_(WS_CS + (gj * 4 + w) * 1920);
    float csr[12];
    const u32 pf2 = opaque((tid < 10 ? tid : 0x10000) * 2048);
    float ra_, rb_, rc_;
    u32 rc1, rms;
    auto mem_pre = [&](auto P_) { if constexpr (SCH.kind[P_] == OP_DMA) set_m0<SCH.arg[P_] * 4096>(m0w); };
    auto mem_op = [&](auto P_) {
      constexpr int kind = SCH.kind[P_], arg = SCH.arg[P_];
      if constexpr (kind == OP_DMA) dma_m0(voff, rht);
      if constexpr (kind == OP_TAB) tab_ld<arg * 256>(reso[arg], rrt, l4, rto);
      if constexpr (kind == OP_PF) tab_ld<0>(cpf, rc, pf2, PF2_FROM + (slot & 15) * 128);
      if constexpr (kind == OP_CS) hld<arg * 640>(csl[arg], rws, cso, cssb);
      if constexpr (kind == OP_W) LDW(W[arg], voff, wp, arg);
      if constexpr (kind == OP_V) { if constexpr (v_in_vgpr(arg)) ldw_v<arg>(V[arg], VOFF_V, vp); else ldv_a<arg>(V[arg], VOFF_V, vp); }
      if constexpr (kind == OP_H) {
        constexpr int m = g2_m(arg >> 1), ks = g2_ks(arg >> 1) + (arg & 1);
        hld_a<(ks & 3) * 1024>(Hf[arg], rws, VOFF_H, hoff + ((m >> 1) * 4 + (m & 1)) * 49152 + (ks >> 2) * 4096);
      }
      if constexpr (kind == OP_POLL) { if constexpr (arg == 0) poll_ld<8192>(fl[0], rws, pollo); else poll_ld<(arg - 1) * 128>(fl[arg], rws, p3); }
      if constexpr (kind == OP_ST) hld<(arg & 3) * 256>(rec[arg >> 2][arg & 3], rws, li16, st2o + (arg >> 2) * 1024);
      if constexpr (kind == OP_PUB) {
        __builtin_amdgcn_raw_buffer_store_b128(u32x4{rc1, tag, rms, tag}, rws, pubv + arg * 1024, 0, SC1);
        asm volatile("" ::: "memory");
      }
      if constexpr (kind == OP_SIG) { vmwait<SCH.cnt[P_] - SCH.cnt[slot0(arg == 0 ? EPIA_G : arg == 1 ? 68 : 80)]>(); add_lane0(rws, z0, sig3 + arg * 128); }
    };
    auto glu = [&](auto H_, auto M_, auto T_) {
      constexpr int hf = H_, m = M_, t = T_;
      const float sd = bfhi(rec[hf][m].z), nm = bflo(rec[hf][m].z), c1 = asf(rec[hf][m].x), s2 = sd * sd;
      const f32x2 ga = __builtin_elementwise_fma(cs[t].xy, f32x2{nm, nm}, acc1[hf][m][t].xy);
      const f32x2 ua = __builtin_elementwise_fma(cs[t].zw, f32x2{nm, nm}, acc1[hf][m][t].zw);
      const f32x2 ar = ga * c1;
      const f32x2 ex = {__builtin_amdgcn_exp2f(ar.x), __builtin_amdgcn_exp2f(ar.y)};
      const f32x2 dn = __builtin_elementwise_fma(ex, f32x2{s2, s2}, f32x2{s2, s2});
      const f32x2 rc = {__builtin_amdgcn_rcpf(dn.x), __builtin_amdgcn_rcpf(dn.y)};
      const f32x2 p = (ga * ua) * rc;
      return pkbf(p.x, p.y);
    };
    auto put4 = [&](u32 a, u32 b2, u32 c, u32 d, u32 off) {
      swap32(a, c); swap32(b2, d); swap16(a, b2); swap16(c, d);
      __builtin_amdgcn_raw_buffer_store_b128(u32x4{a, b2, c, d}, rws, off, 0, SC1);
      asm volatile("" ::: "memory");
    };
    auto swiglu_part = [&](auto P_) {
      constexpr int p = P_;
      asm volatile("s_nop 7" ::: "memory");
      SCHED();
      put4(glu(IC<1>{}, IC<p>{}, IC<0>{}), glu(IC<1>{}, IC<p + 2>{}, IC<0>{}), glu(IC<1>{}, IC<p>{}, IC<1>{}), glu(IC<1>{}, IC<p + 2>{}, IC<1>{}), hidB + p * 49152);
      u32 a = glu(IC<1>{}, IC<p>{}, IC<2>{}), b2 = glu(IC<1>{}, IC<p + 2>{}, IC<2>{});
      put4(a, b2, a, b2, hidC + p * 49152);
      SCHED();
    };
    auto checks = [&](auto G_) {
      constexpr int g = G_;
      if constexpr (g == CHKA_G) { STAMP_AT(4); poll_spin<0, SCH.wait(g, OP_POLL, 1), false>(fl[1], rws, p3, c16); STAMP_AT(18); }
      if constexpr (g == CHKB_G) { GSTAMP(25); poll_spin<128, SCH.wait(g, OP_POLL, 2), false>(fl[2], rws, p3, c16); GSTAMP(26); }
      if constexpr (g == CHKC_G) { STAMP_AT(11); poll_spin<256, SCH.wait(g, OP_POLL, 3), false>(fl[3], rws, p3, c16); STAMP_AT(20); }
    };
    STAMP_AT(21);
    vmwait<NPRE - 3>();
    __builtin_amdgcn_s_barrier();
    asm volatile("" ::: "memory");
    STAMP_AT(22);
    sfor<0, 6>([&](auto F_) { rdb(F_, IC<0>{}); rdb(F_, IC<1>{}); });
    sfor<0, 80>([&](auto G_) {
      constexpr int g = G_, f = 2 * g, hf = g1_half(g), m = g1_m(g), ks = g1_ks(g);
      if constexpr (hf == 0 && m == 0 && 3 * ks + 5 >= NW_CONV) vmwait<SCH.wait(g, OP_W, 3 * ks + 5)>();
      if constexpr (g == 32) STAMP_AT(3);
      if constexpr (g == EPIA_G) {
        STAMP_AT(31);
        rec_check<SCH.wait(g, OP_ST, 3, 3)>(rec[0], tag, rws, li16, st2o);
        SCHED();
        sfor<0, 3>([&](auto T) { put4(glu(IC<0>{}, IC<0>{}, T), glu(IC<0>{}, IC<1>{}, T), glu(IC<0>{}, IC<2>{}, T), glu(IC<0>{}, IC<3>{}, T), hidA + T * 1024); });
        SCHED();
        STAMP_AT(23);
      }
      if constexpr (g == 68) { GSTAMP(27); rec_check<SCH.wait(g, OP_ST, 7, 2)>(rec[1], tag, rws, li16, st2o + 1024); swiglu_part(IC<0>{}); GSTAMP(14); }
      if constexpr (g == CHECK_G) { STAMP_AT(24); poll_spin<8192, SCH.wait(g, OP_POLL, 0), false>(fl[0], rws, pollo, c32); STAMP_AT(29); sfor<0, 3>([&](auto T) { tie(csl[T]); }); }
      checks(G_);
      if constexpr ((g & 1) == 0 || g == SA_G || g == SB_G) lgkwait<(g < 78 ? 4 : 0)>();
      if constexpr (g == 5) { vmwait<SCH.wait(g, OP_DMA, 7)>(); __builtin_amdgcn_s_barrier(); }
      if constexpr (g == 12) { vmwait<SCH.wait(g, OP_DMA, 15)>(); __builtin_amdgcn_s_barrier(); }
      if constexpr (g == 29 && LANDA_G > 29) { vmwait<SCH.wait(g, OP_DMA, 15 + 2 * ((LANDA_G - 29 + 3) / 4))>(); __builtin_amdgcn_s_barrier(); }
      if constexpr (g == LANDA_G) { vmwait<SCH.wait(g, OP_DMA, 27)>(); __builtin_amdgcn_s_barrier(); }
      if constexpr (g == LAND3_G) { vmwait<SCH.wait(g, OP_DMA, 39)>(); __builtin_amdgcn_s_barrier(); }
      if constexpr (g == SA_G || g == SB_G) __builtin_amdgcn_s_barrier();
      asm volatile("" ::: "memory");
      if constexpr (g == SA_G || g == SB_G) { pa[0] = *(LAS f32x4*)(strd + (g == SB_G) * 1024); pa[1] = *(LAS f32x4*)(strd + (g == SB_G) * 1024 + 16); }
      SCHED();
      const u32x4 B[2] = {{Bl[f % 10].x, Bl[f % 10].y, Bh[f % 10].x, Bh[f % 10].y}, {Bl[(f + 1) % 10].x, Bl[(f + 1) % 10].y, Bh[(f + 1) % 10].x, Bh[(f + 1) % 10].y}};
      constexpr int sh = g == 10 || g == LANDA_G ? 0 : g == 12 || g == LAND3_G ? 1 : -1, si0 = g < 16 ? 0 : 2, sn = g < 16 ? 2 : 3;
      constexpr int mh = g == 11 || g == LANDA_G + 1 ? 0 : g == 13 || g == LAND3_G + 1 ? 1 : -1;
      sfor<0, 6>([&](auto N_) {
        constexpr int n = N_, c = n >> 1, t = (g & 1) ? 2 - c : c, kk = (n ^ c ^ g) & 1;
        mf_bfa<ks == 0 && (n & 1) == 0>(acc1[hf][m][t], W[(ks + kk) * 3 + t], B[kk]);
        if constexpr (sh >= 0 && (n & 1) == 0 && c < sn) {
          constexpr int o = hpos(sh < 0 ? 0 : sh, 4 * (si0 + c)) * 4096;
          Sf[5 * sh + si0 + c] = trfrag(sb[o >> 16] + (o & 65535), sb[o >> 16] + (o & 65535) + 2048);
        }
        if constexpr (n == 0) { rdb(IC<f + 6>{}, IC<0>{}); mem_pre(IC<f>{}); }
        if constexpr (n == 1) rdb(IC<f + 6>{}, IC<1>{});
        if constexpr (n == 2) mem_op(IC<f>{});
        if constexpr (n == 3) { rdb(IC<f + 7>{}, IC<0>{}); mem_pre(IC<f + 1>{}); }
        if constexpr (n == 4) rdb(IC<f + 7>{}, IC<1>{});
        if constexpr (n == 5) mem_op(IC<f + 1>{});
        if constexpr (g == LANDA_G + 2) xor_in(reso[n], rsx);
        if constexpr (g >= CSR_G && g < CSR_G + 8) {
          constexpr int e = (6 * (g - CSR_G) + n) % 12, st = (6 * (g - CSR_G) + n) / 12;
          if constexpr (st == 0) csr[e] = asf(csl[e >> 2][e & 3]);
          dpp_step<st>(csr[e]);
          if constexpr (st == 3) cs[e >> 2][e & 3] = csr[e];
        }
        if constexpr (g == SA_G + 1 || g == SB_G + 1) rec_step<n>(pa, ra_, rb_, rc_, rc1, rms);
        if constexpr ((g == SA_G + 2 || g == SB_G + 2) && n < 4) rec_step<6 + n>(pa, ra_, rb_, rc_, rc1, rms);
        if constexpr (g == SB_G + 3) res[n] = tr64((LAS char*)(size_t)reso[n]);
        SCHED();
      });
      if constexpr (mh >= 0) {
        sfor<0, sn>([&](auto C) { mf_bfv<si0 + C == 0>(sq[mh], Sf[5 * mh + si0 + C], Sf[5 * mh + si0 + C]); });
        sfor<0, sn>([&](auto C) { mf_bfv<si0 + C == 0>(su[mh], on, Sf[5 * mh + si0 + C]); });
      }
      if constexpr (g == SA_G - 2 || g == SB_G - 2) {
        constexpr int h = g == SB_G - 2;
        asm volatile("" ::"v"(su[h]));
        const int e = li & 3;
        *(LAS f32x2*)(stwr + h * 1024) = f32x2{e == 0 ? sq[h][0] : (e == 1 ? sq[h][1] : (e == 2 ? sq[h][2] : sq[h][3])), su[h][0]};
      }
      SCHED();
    });
    STAMP_AT(9);
    if constexpr (PF2_G >= 0) tie(cpf);
    __builtin_amdgcn_s_barrier();
    swiglu_part(IC<1>{});
    STAMP_AT(10);
    const rsrc_t ro = __builtin_amdgcn_make_buffer_rsrc(out, 0, L_ * D_ * 2, 0x00020000);
    auto putp = [&](auto F_, auto E_) { constexpr int f = F_, t = E_ / 4, r = E_ % 4; put1<(f < 2 ? f * 25600 : f * 25600 - 40000) + t * 1280 + r * 272>(acc2[f][t][r]); };
    auto finalize = [&](auto H_) {
      constexpr int h = H_;
      BARRIER();
      f32x4 pv[3][4];
#pragma unroll
      for (int v = 0; v < 4; v++) {
        pv[0][v] = *(LAS f32x4*)(redw + h * 51200 + v * 6400); pv[1][v] = *(LAS f32x4*)(redw + h * 51200 + v * 6400 + 16);
        pv[2][v] = *(LAS f32x4*)(redv + h * 51200 + v * 6400);
      }
      SCHED();
      u32 o[3][2];
#pragma unroll
      for (int k = 0; k < 3; k++) {
        const u32x2 rr = res[3 * h + k];
        const f32x4 v = ((pv[k][0] + f32x4{bflo(rr.x), bfhi(rr.x), bflo(rr.y), bfhi(rr.y)}) + pv[k][1]) + (pv[k][2] + pv[k][3]);
        o[k][0] = pkbf(v[0], v[1]); o[k][1] = pkbf(v[2], v[3]);
      }
      __builtin_amdgcn_raw_buffer_store_b128(u32x4{o[0][0], o[0][1], o[1][0], o[1][1]}, ro, outo + h * (64 * D_ * 2), 0, OUT_AUX);
      __builtin_amdgcn_raw_buffer_store_b64(u32x2{o[2][0], o[2][1]}, ro, outv + h * (64 * D_ * 2), 0, OUT_AUX);
      asm volatile("" ::: "memory");
      SCHED();
    };
    sfor<0, 24>([&](auto G_) {
      constexpr int g = G_, m = g2_m(g), ks = g2_ks(g), wh = SCH.wait(80 + g, OP_H, 2 * g + 1), wv = SCH.wait(80 + g, OP_V, ks * 5 + 9);
      if constexpr (80 + g == FINA_G) finalize(IC<0>{});
      checks(IC<80 + g>{});
      vmwait<(wh < wv ? wh : wv)>();
      if constexpr (g == 12) set_m0<0>(w * 6400);
      if constexpr (g == 19) set_m0<40000>(w * 6400);
      sfor<0, 10>([&](auto N_) {
        constexpr int n = N_, c = n >> 1, t = (g & 1) ? 4 - c : c, kk = (n ^ c ^ g) & 1;
        if constexpr (v_in_vgpr((ks + kk) * 5 + t)) mf_va<ks == 0 && (n & 1) == 0>(acc2[m][t], V[(ks + kk) * 5 + t], Hf[2 * g + kk]);
        else mf_aa<ks == 0 && (n & 1) == 0>(acc2[m][t], V[(ks + kk) * 5 + t], Hf[2 * g + kk]);
        if constexpr (n % 3 == 1) mem_op(IC<slot0(80 + g) + n / 3>{});
        if constexpr (g >= 12 && g < 16) putp(IC<(g - 12) / 2>{}, IC<10 * (g & 1) + n>{});
        if constexpr (g == 19 || g == 20) putp(IC<2>{}, IC<10 * (g - 19) + n>{});
        if constexpr (g == 23 && n == 9) sfor<16, 20>([&](auto E) { putp(IC<3>{}, E); });
      });
      SCHED();
    });
    sfor<0, 16>([&](auto E) { putp(IC<3>{}, IC<4 * (3 - E / 4) + E % 4>{}); });
    STAMP_AT(12);
    finalize(IC<1>{});
  }
  if (b == 0 && tid == 0) __builtin_amdgcn_raw_buffer_store_b32(tag, rws, WS_EPOCH, 0, SC1);
  asm volatile(".Lfk_end:");
  STAMP_AT(13);
}

extern "C" void kernel_launch(void* const* d_in, const int* in_sizes, int n_in, void* d_out, int out_size, void* d_ws, size_t ws_size,
                              hipStream_t stream) {
  (void)hipFuncSetAttribute((const void*)fused_kernel, hipFuncAttributeMaxDynamicSharedMemorySize, LDS_BYTES);
  fused_kernel<<<256, 256, LDS_BYTES, stream>>>((const u16*)d_in[0], (const u16*)d_in[1], (const u16*)d_in[2], (const u16*)d_in[3], (const u16*)d_in[4],
      (const u16*)d_in[5], (const u16*)d_in[6], (const u16*)d_in[8], (const u16*)d_in[9], (const u16*)d_in[10], (u16*)d_out, (char*)d_ws + (ws_size >= WS_BASE + WS_BYTES ? WS_BASE : 0));
}
```
